```python
import torch

def _softmax(x, axis=-1):
    return torch.softmax(x, dim=axis)
def _gelu(x):
    return torch.nn.functional.gelu(x, approximate="tanh")
def _silu(x):
    return torch.nn.functional.silu(x)
def _sigmoid(x):
    return torch.sigmoid(x)
def _layernorm(x, axis=-1, eps=1e-5):
    m = x.mean(dim=axis, keepdim=True)
    v = x.var(dim=axis, keepdim=True, unbiased=False)
    return (x - m) / torch.sqrt(v + eps)
def _scatter_add(tgt, idx, src, axis=0):
    return tgt.clone().index_add_(axis, idx, src)
def _segment_sum(src, seg, n):
    out = torch.zeros((n,) + src.shape[1:], dtype=src.dtype)
    return out.index_add_(0, seg, src)
def _pad(x, pw):
    flat = tuple(v for pair in reversed(pw) for v in pair)
    return torch.nn.functional.pad(x, flat)

kernel_name = 'gated_mix__sliding_block_cached__dense_activation_mish__decode'

def setup_inputs():

    _w = (lambda a: (lambda t: t.to(torch.bfloat16) if t.is_floating_point() else t)(torch.as_tensor(a)))
    def _A_setup():
        B, Sq, D = 1, 1, 512
        Hq, Hkv, Sk, Dh = 32, 8, 65536, 128
        assert Hq % Hkv == 0, (Hq, Hkv)
        return {
            "x": _w((lambda *s: torch.randn(*s, dtype=torch.bfloat16) * 0.05)(B, Sq, D)),
            "k_cache": _w((lambda *s: torch.randn(*s, dtype=torch.bfloat16) * 6.4)(B, Hkv, Sk, Dh)),
            "v_cache": _w((lambda *s: torch.randn(*s, dtype=torch.bfloat16) * 0.4)(B, Hkv, Sk, Dh)),
            "wq": _w((lambda *s: torch.randn(*s, dtype=torch.bfloat16) * 6.4)(D, Hq * Dh) * (D ** -0.5)),
            "wo": _w((lambda *s: torch.randn(*s, dtype=torch.bfloat16) * 0.4)(Hq * Dh, D) * ((Hq * Dh) ** -0.5)),
        }
    _A_in = _A_setup()

    def _B_setup():
        B, S, D, F = 1, 1, 512, 1792
        return {
            "x": _w((lambda *s: torch.randn(*s, dtype=torch.bfloat16) * 0.05)(B, S, D)),
            "w1": _w((lambda *s: torch.randn(*s, dtype=torch.bfloat16) * 12.8)(D, F) * (D ** -0.5)),
            "b1": _w((lambda *s: torch.randn(*s, dtype=torch.bfloat16) * 0.05)(F)),
            "w2": _w((lambda *s: torch.randn(*s, dtype=torch.bfloat16) * 0.05)(F, D) * (F ** -0.5)),
        }
    _B_in = _B_setup()

    return {
        "A_x": _A_in["x"],
        "A_k_cache": _A_in["k_cache"],
        "A_v_cache": _A_in["v_cache"],
        "A_wq": _A_in["wq"],
        "A_wo": _A_in["wo"],
        "B_x": _B_in["x"],
        "B_w1": _B_in["w1"],
        "B_b1": _B_in["b1"],
        "B_w2": _B_in["w2"],
        "gate_w": _w((lambda *s: torch.randn(*s, dtype=torch.bfloat16) * 0.05)(512) * 16.0),
    }

def reference(A_x, A_k_cache, A_v_cache, A_wq, A_wo, B_x, B_w1, B_b1, B_w2, gate_w):
    def _A_ref(x, k_cache, v_cache, wq, wo):
        B, Sq, D = x.shape
        Hq, Dh = 32, 128
        Hkv, Sk = k_cache.shape[1], k_cache.shape[2]
        W = min(4096, max(256, Sk // 4))
        rep = Hq // Hkv
        q = (lambda x, *d: x.permute(*d))((x @ wq).reshape(B, Sq, Hq, Dh), 0, 2, 1, 3)
        k = (lambda x, n, axis: torch.repeat_interleave(x, n, dim=axis))(k_cache[:, :, Sk - W:, :], rep, axis=1)
        v = (lambda x, n, axis: torch.repeat_interleave(x, n, dim=axis))(v_cache[:, :, Sk - W:, :], rep, axis=1)
        scores = torch.einsum("bhqd,bhkd->bhqk", q, k) * (Dh ** -0.5)
        p = _softmax(scores, axis=-1)
        o = torch.einsum("bhqk,bhkd->bhqd", p, v)
        return (lambda x, *d: x.permute(*d))(o, 0, 2, 1, 3).reshape(B, Sq, Hq * Dh) @ wo

    def _B_ref(x, w1, b1, w2):
        h = torch.einsum("bsd,df->bsf", x, w1) + b1
        sp = torch.log1p(torch.exp(-torch.abs(h))) + torch.where(h > 0, h, torch.zeros_like(h))
        act = h * torch.tanh(sp)
        return torch.einsum("bsf,fd->bsd", act, w2)

    x = A_x
    g = _sigmoid((x * gate_w).sum(-1, keepdims=True))
    a = _A_ref(x, A_k_cache, A_v_cache, A_wq, A_wo)
    b = _B_ref(x, B_w1, B_b1, B_w2)
    return g * a + (1 - g) * b
```

```cpp
#include <hip/hip_runtime.h>
#include <hip/hip_bf16.h>
typedef unsigned short u16;
__device__ __forceinline__ float bf2f(u16 v){ return __uint_as_float(((unsigned)v)<<16); }

__global__ __launch_bounds__(64) void k1(const u16* x, const u16* wq, const u16* w1, const u16* b1, float* q, float* act){
  int n = blockIdx.x*blockDim.x+threadIdx.x;
  if(n<4096){ float s=0; for(int d=0;d<512;d++) s+=bf2f(x[d])*bf2f(wq[d*4096+n]); q[n]=s; }
  else if(n<4096+1792){ int f=n-4096; float s=0; for(int d=0;d<512;d++) s+=bf2f(x[d])*bf2f(w1[d*1792+f]); s+=bf2f(b1[f]);
    float sp=log1pf(expf(-fabsf(s)))+fmaxf(s,0.f); act[f]=s*tanhf(sp);} }
__global__ __launch_bounds__(128) void k2(const float* q, const u16* K, const u16* V, float* po, float* pm, float* pl){
  int h=blockIdx.x/32, s=blockIdx.x%32, g=h/4, t=threadIdx.x;
  __shared__ float sc[128]; __shared__ float red[128];
  const u16* kp = K + ((size_t)g*65536 + 65536-4096 + s*128 + t)*128;
  float a=0; for(int d=0;d<128;d++) a+=q[h*128+d]*bf2f(kp[d]);
  a*=0.08838834764831845f; red[t]=a; __syncthreads();
  float m=-1e30f; for(int i=0;i<128;i++) m=fmaxf(m,red[i]);
  float p=expf(a-m); sc[t]=p; __syncthreads();
  float l=0; for(int i=0;i<128;i++) l+=sc[i];
  const u16* vp = V + ((size_t)g*65536 + 65536-4096 + s*128)*128;
  float o=0; for(int i=0;i<128;i++) o+=sc[i]*bf2f(vp[i*128+t]);
  po[(h*32+s)*128+t]=o; if(t==0){pm[h*32+s]=m; pl[h*32+s]=l;}
}
__global__ __launch_bounds__(512) void k3(const float* po,const float* pm,const float* pl,const u16* wo,const float* act,const u16* w2,float* part){
  int b=blockIdx.x,t=threadIdx.x; __shared__ float o[128];
  if(b<32){
    if(t<128){ float M=-1e30f; for(int s=0;s<32;s++) M=fmaxf(M,pm[b*32+s]); float L=0,acc=0; for(int s=0;s<32;s++){ float w=expf(pm[b*32+s]-M); L+=w*pl[b*32+s]; acc+=w*po[(b*32+s)*128+t]; } o[t]=acc/L; }
    __syncthreads();
    float s=0; for(int d=0;d<128;d++) s+=o[d]*bf2f(wo[(b*128+d)*512+t]); part[b*512+t]=s;
  } else { int c=b-32; float s=0; for(int d=0;d<128;d++) s+=act[c*128+d]*bf2f(w2[(c*128+d)*512+t]); part[b*512+t]=s; }
}
__global__ __launch_bounds__(512) void k4(const float* part,const u16* x,const u16* gw,__hip_bfloat16* out){
  int t=threadIdx.x; float g=0; for(int d=0;d<512;d++) g+=bf2f(x[d])*bf2f(gw[d]); g=1.f/(1.f+expf(-g));
  float a=0,b=0; for(int i=0;i<32;i++) a+=part[i*512+t]; for(int i=32;i<46;i++) b+=part[i*512+t];
  out[t]=__float2bfloat16(g*a+(1-g)*b);
}
extern "C" void kernel_launch(void* const* d_in, const int* in_sizes, int n_in, void* d_out, int out_size, void* d_ws, size_t ws_size, hipStream_t stream) {
  const u16* x=(const u16*)d_in[0]; const u16* K=(const u16*)d_in[1]; const u16* V=(const u16*)d_in[2];
  const u16* wq=(const u16*)d_in[3]; const u16* wo=(const u16*)d_in[4];
  const u16* w1=(const u16*)d_in[6]; const u16* b1=(const u16*)d_in[7]; const u16* w2=(const u16*)d_in[8]; const u16* gw=(const u16*)d_in[9];
  float* ws=(float*)d_ws; float* q=ws; float* act=ws+4096; float* po=ws+8192; float* pm=po+32*32*128; float* pl=pm+1024; float* part=pl+1024;
  k1<<<(4096+1792)/64,64,0,stream>>>(x,wq,w1,b1,q,act);
  k2<<<1024,128,0,stream>>>(q,K,V,po,pm,pl);
  k3<<<46,512,0,stream>>>(po,pm,pl,wo,act,w2,part);
  k4<<<1,512,0,stream>>>(part,x,gw,(__hip_bfloat16*)d_out);
}
```

```cpp
#include <hip/hip_runtime.h>
#include <hip/hip_bf16.h>

#define DEPTH 24
#define QX 0
#define QPOLL_AT 40
#define POL 2
#define STAMPS 0

#define LAS __attribute__((address_space(3)))
typedef unsigned u32;
typedef unsigned long long u64;
typedef u32 v4u __attribute__((ext_vector_type(4)));
typedef u32 v2u __attribute__((ext_vector_type(2)));
typedef float v4f __attribute__((ext_vector_type(4)));
typedef float v2f __attribute__((ext_vector_type(2)));
typedef short v4s __attribute__((ext_vector_type(4)));
typedef __bf16 v8b __attribute__((ext_vector_type(8)));
typedef __bf16 v2b __attribute__((ext_vector_type(2)));
typedef __amdgpu_buffer_rsrc_t rsrc_t;

constexpr int PB = 1056;
constexpr int L_X = 0, L_GW = 1024, L_MISC = 2048, L_DUMMY = 3072;
constexpr int L_WQ = 4096, L_K = L_WQ + 16384, L_V = L_K + 32 * PB, L_W1 = L_V + 32 * PB, L_WO = L_W1 + 8192, L_W2 = L_WO + 16 * PB;
constexpr int L_Q = L_W2 + 8 * PB, L_P = L_Q + 1280, L_ML = L_P + 4096, L_WT = L_ML + 128, L_O = L_WT + 128, L_ACT = L_O + 128, L_FLAG = L_ACT + 64;
constexpr int L_END = L_FLAG + 64;
enum { F_PROG = 0, F_Q = 1, F_LSE = 2, F_OPUB = 3, F_P0 = 4, F_ACT = 8, F_O = 9, F_G = 10 };
constexpr int W_H1 = 64, W_LSE = 576, W_O = 832, W_HF = 2880, W_H3 = 3328, W_LINES = 4352;
constexpr int WS_BYTES = (W_LINES / 64) * 16384;
__device__ __forceinline__ u32 wline(u32 L) { return (L >> 6) * 16384 + (L & 63) * 128; }
constexpr int M_WQ = 20, M_K = 20, M_V = 52, M_W1 = 92, M_ALL = 127;
constexpr u32 SPIN_CAP = 1u << 18;

__device__ __forceinline__ rsrc_t mk(const void* p, u32 bytes) { return __builtin_amdgcn_make_buffer_rsrc((void*)p, 0, bytes, 0x00020000); }
__device__ __forceinline__ v4f mfma(v4u a, v4u b, v4f c) { return __builtin_amdgcn_mfma_f32_16x16x32_bf16(__builtin_bit_cast(v8b, a), __builtin_bit_cast(v8b, b), c, 0, 0, 0); }
__device__ __forceinline__ u32 pk(float lo, float hi) { v2f f = {lo, hi}; return __builtin_bit_cast(u32, __builtin_convertvector(f, v2b)); }
__device__ __forceinline__ float asf(u32 x) { return __builtin_bit_cast(float, x); }
__device__ __forceinline__ u32 asu(float x) { return __builtin_bit_cast(u32, x); }
template <int C> __device__ __forceinline__ float dpp(float x) { return asf(__builtin_amdgcn_mov_dpp(asu(x), C, 0xf, 0xf, true)); }
constexpr int XOR1 = 0xB1, XOR2 = 0x4E, XOR7 = 0x141, ROR8 = 0x128, ROR4 = 0x124, ROR2 = 0x122;
__device__ __forceinline__ float sum16(float a, float b) { auto r = __builtin_amdgcn_permlane16_swap(asu(a), asu(b), false, false); return asf(r[0]) + asf(r[1]); }
__device__ __forceinline__ float sum32(float a, float b) { auto r = __builtin_amdgcn_permlane32_swap(asu(a), asu(b), false, false); return asf(r[0]) + asf(r[1]); }
__device__ __forceinline__ float max16(float a) { auto r = __builtin_amdgcn_permlane16_swap(asu(a), asu(a), false, false); return fmaxf(asf(r[0]), asf(r[1])); }
__device__ __forceinline__ float max32(float a) { auto r = __builtin_amdgcn_permlane32_swap(asu(a), asu(a), false, false); return fmaxf(asf(r[0]), asf(r[1])); }
__device__ __forceinline__ float dot2(u32 a, u32 b, float c) { return __builtin_amdgcn_fdot2_f32_bf16(__builtin_bit_cast(v2b, a), __builtin_bit_cast(v2b, b), c, false); }
#define FENCE() asm volatile("" ::: "memory")

#define ST(i) do {} while (0)

template <int NT> __device__ __forceinline__ void dma(u32 lds, u32 voff, rsrc_t r, u32 soff) {
  if (NT == 1) asm volatile("s_mov_b32 m0, %0\n\ts_nop 0\n\tbuffer_load_dwordx4 %1, %2, %3 offen nt lds" ::"s"(lds), "v"(voff), "s"(r), "s"(soff) : "memory");
  else if (NT == 2) asm volatile("s_mov_b32 m0, %0\n\ts_nop 0\n\tbuffer_load_dwordx4 %1, %2, %3 offen sc1 lds" ::"s"(lds), "v"(voff), "s"(r), "s"(soff) : "memory");
  else asm volatile("s_mov_b32 m0, %0\n\ts_nop 0\n\tbuffer_load_dwordx4 %1, %2, %3 offen lds" ::"s"(lds), "v"(voff), "s"(r), "s"(soff) : "memory");
}
template <int LP, int SP> __device__ __forceinline__ void dma4(u32 lds, u32 voff, rsrc_t r, u32 soff) {
  asm volatile("s_mov_b32 m0, %1\n\ts_nop 0\n\tbuffer_load_dwordx4 %2, %3, %0 offen nt lds\n\t"
               "s_add_u32 m0, m0, %4\n\ts_add_u32 %0, %0, %5\n\tbuffer_load_dwordx4 %2, %3, %0 offen nt lds\n\t"
               "s_add_u32 m0, m0, %4\n\ts_add_u32 %0, %0, %5\n\tbuffer_load_dwordx4 %2, %3, %0 offen nt lds\n\t"
               "s_add_u32 m0, m0, %4\n\ts_add_u32 %0, %0, %5\n\tbuffer_load_dwordx4 %2, %3, %0 offen nt lds"
               : "+s"(soff) : "s"(lds), "v"(voff), "s"(r), "n"(LP), "n"(SP) : "memory", "scc");
}
template <int N> __device__ __forceinline__ void prog(u32 flag_addr, int v) {
  asm volatile("s_waitcnt vmcnt(%2)\n\tds_write_b32 %0, %1" ::"v"(flag_addr), "v"(v), "n"(N) : "memory");
}
template <int K> __device__ __forceinline__ void drain(u32 fa, int n) {
  if constexpr (K > 0) { prog<K>(fa, n - K); drain<K - 4>(fa, n); }
}

__global__ __launch_bounds__(512, 2) void fused(const void* __restrict__ gx, const void* __restrict__ gK, const void* __restrict__ gV,
                                                const void* __restrict__ gwq, const void* __restrict__ gwo, const void* __restrict__ gw1,
                                                const void* __restrict__ gb1, const void* __restrict__ gw2, const void* __restrict__ ggw,
                                                void* __restrict__ gout, void* __restrict__ gws) {
  __shared__ __attribute__((aligned(16))) char smem[L_END];
  LAS char* const S = (LAS char*)smem;
  const u32 sb = (u32)(size_t)S;
  volatile LAS int* const flag = (volatile LAS int*)(S + L_FLAG);
  const u32 x = blockIdx.x & 7, s = blockIdx.x >> 3;
  const u32 lane = threadIdx.x & 63, wv = __builtin_amdgcn_readfirstlane(threadIdx.x >> 6);
  const bool ffn = s < 28;
  const u32 c = x >> 1, gc = 2 * (s >> 3) + ((x & 1) ^ 1), hh = s & 7;
  auto wait = [&](int f, int v) { while (flag[f] < v) { __builtin_amdgcn_s_sleep(1); } FENCE(); };
  auto post = [&](int f) { FENCE(); flag[f] = 1; };

  if (__builtin_expect(wv == 0, 1)) {
    __builtin_amdgcn_s_setprio(3);
    const u32 fa = sb + L_FLAG;
    const u32 v16 = lane * 16;
    dma<0>(sb + L_X, v16, mk(gx, 1024), 0);
    const u32 r8 = lane >> 3, sw8 = ((lane & 7) ^ (r8 & 6)) * 16;
    const u32 r4 = lane >> 4, sw4 = ((lane & 15) ^ (4 * r4)) * 16;
    const rsrc_t rq = mk(gwq, 512 * 4096 * 2);
    const u32 vq = r8 * 8192 + sw8;
    u32 so = (s >> 3) * (128 * 8192) + (x ^ QX) * 1024 + (s & 7) * 128, la = sb + L_WQ;
#pragma unroll 1
    for (int i = 0; i < 4; i++) { dma4<1024, 8 * 8192>(la, vq, rq, so); la += 4096; so += 32 * 8192; }
    dma<2>(sb + L_MISC, v16, mk(gws, 128), 0);
    dma<0>(sb + L_GW, v16, mk(ggw, 1024), 0);
    dma<0>(sb + L_DUMMY, v16, mk(gb1, 3584), s * 128 + (x & 1) * 64);
    ST(1);
    __builtin_amdgcn_s_barrier();
    int n = 20;
    const u32 vk = r4 * 256 + sw4;
    const u32 kv0 = (x * 65536 + 61440 + s * 128) * 256;
    la = sb + L_K;
#pragma unroll 1
    for (int t = 0; t < 2; t++) {
      const rsrc_t r = mk(t ? gV : gK, 8u * 65536 * 256);
#pragma unroll 1
      for (int i = 0; i < 32; i += 4) { dma4<PB, 1024>(la, vk, r, kv0 + ((i ^ ((s & 3) << 3)) << 10)); la += 4 * PB; n += 4; prog<DEPTH>(fa, n - DEPTH); }
    }
    ST(2);
    if (ffn) {
      const rsrc_t r1 = mk(gw1, 512 * 1792 * 2);
      const u32 v1 = r8 * 3584 + sw8;
      so = x * (64 * 3584) + s * 128;
#pragma unroll 1
      for (int i = 0; i < 2; i++) { dma4<1024, 8 * 3584>(la, v1, r1, so); la += 4096; so += 32 * 3584; n += 4; prog<DEPTH>(fa, n - DEPTH); }
    }
    const u32 vo = r4 * 1024 + sw4;
    const rsrc_t ro = mk(gwo, 4096 * 512 * 2);
    la = sb + L_WO;
    so = (gc * 512 + (hh & 1) * 256 + (hh >> 1) * 32) * 1024 + c * 256;
#pragma unroll 1
    for (int i = 0; i < 4; i++) { dma4<PB, 4096>(la, vo, ro, so + (i & 1) * 16384 + (i >> 1) * (128 * 1024)); la += 4 * PB; n += 4; prog<DEPTH>(fa, n - DEPTH); }
    if (ffn) {
      const rsrc_t r2 = mk(gw2, 1792 * 512 * 2);
      so = (s * 64 + (x & 1) * 32) * 1024 + c * 256;
#pragma unroll 1
      for (int i = 0; i < 2; i++) { dma4<PB, 4096>(la, vo, r2, so); la += 4 * PB; so += 16384; n += 4; prog<DEPTH>(fa, n - DEPTH); }
    }
    ST(3);
    drain<DEPTH - 4>(fa, n);
    prog<0>(fa, M_ALL);
    ST(4);
  } else {
    const rsrc_t rw = mk(gws, WS_BYTES);
    const u32 col = lane & 15, kg = lane >> 4, q4 = col >> 2, p4 = col & 3;
    const u32 cp = (POL == 2) ? 0 : ((((x >> 2) & 1) ^ POL) * 8192);
    if (wv == 3 && lane < 16) flag[lane] = 0;
    u32 o_off = 0;
    if (wv >= 4) {
      const u32 w = wv - 4;
      o_off = wline(W_O + (x * 8 + 2 * w + (p4 >> 1)) * 32 + s) + (col >> 3) * 8192 + (col & 1) * 64 + (q4 & 1) * 32 + kg * 8;
      v2u ones = {~0u, ~0u};
      __builtin_amdgcn_raw_buffer_store_b64(ones, rw, o_off, 0, 16);
    }
    asm volatile("s_waitcnt lgkmcnt(0)" ::: "memory");
    __builtin_amdgcn_s_barrier();
    FENCE();
    wait(F_PROG, M_WQ);
    const u32 ep = __builtin_amdgcn_readfirstlane(*(LAS u32*)(S + L_MISC)) + 1;
    const u32 tag = ep & 0xff;
    ST(1);
    if (wv <= 2) {
      const bool isq = wv == 1;
      if (!isq) {
        const v4u a = *(LAS v4u*)(S + L_X + lane * 16), b = *(LAS v4u*)(S + L_GW + lane * 16);
        float d = 0.f;
#pragma unroll
        for (int i = 0; i < 4; i++) d = dot2(a[i], b[i], d);
        d += dpp<XOR1>(d); d += dpp<XOR2>(d); d += dpp<ROR4>(d); d += dpp<ROR8>(d); d = sum16(d, d); d = sum32(d, d);
        const float g = __builtin_amdgcn_rcpf(1.f + __builtin_amdgcn_exp2f(-1.4426950408889634f * d));
        *(LAS float*)(S + L_FLAG + 4 * F_G) = g;
        if (!ffn) { post(F_ACT); goto done; }
        wait(F_PROG, M_W1);
      }
      {
        const u32 rr = 4 * kg + q4, c6 = rr & 6;
        LAS char* wb = S + (isq ? L_WQ : L_W1) + rr * 128 + 8 * (p4 & 1) + (p4 >> 1) * 16;
        LAS char* xb = S + L_X + (isq ? (s >> 3) * 256 : x * 128) + kg * 8;
        v4f acc[4] = {};
        const int nks = isq ? 4 : 2;
#pragma unroll 1
        for (int ks = 0; ks < nks; ks++) {
          const v2u x0 = *(LAS v2u*)(xb), x1 = *(LAS v2u*)(xb + 32);
          const v4u bx = {x0[0], x0[1], x1[0], x1[1]};
#pragma unroll
          for (int u = 0; u < 4; u++) {
            LAS char* pa = wb + ((2 * u) ^ c6) * 16;
            const v2u a0 = __builtin_bit_cast(v2u, __builtin_amdgcn_ds_read_tr16_b64_v4i16((LAS v4s*)(pa)));
            const v2u a1 = __builtin_bit_cast(v2u, __builtin_amdgcn_ds_read_tr16_b64_v4i16((LAS v4s*)(pa + 2048)));
            acc[u] = mfma(v4u{a0[0], a0[1], a1[0], a1[1]}, bx, acc[u]);
          }
          wb += 4096; xb += 64;
        }
        v4u o;
#pragma unroll
        for (int r = 0; r < 4; r++) {
          const float v = (p4 & 2) ? ((p4 & 1) ? acc[3][r] : acc[2][r]) : ((p4 & 1) ? acc[1][r] : acc[0][r]);
          o[r] = (asu(v) & ~0xffu) | tag;
        }
        const u32 L = isq ? W_H1 + (x ^ QX) * 64 + s * 2 + (p4 >> 1) : W_HF + (s * 2 + (p4 >> 1)) * 8 + x;
        if (col < 8) __builtin_amdgcn_raw_buffer_store_b128(o, rw, wline(L) + q4 * 8192 + (p4 & 1) * 64 + kg * 16, 0, 16);
      }
      ST(2);
      if (isq) {
        wait(F_PROG, QPOLL_AT);
        ST(3);
        const u32 off = wline(W_H1 + x * 64) + cp + lane * 16;
        v4u d[8];
_Pragma("unroll 1") for (u32 it = 0; it < SPIN_CAP; it++) {
          u32 bad = 0;
#pragma unroll
          for (int i = 0; i < 8; i++) d[i] = __builtin_amdgcn_raw_buffer_load_b128(rw, off, i * 1024, 16);
#pragma unroll
          for (int i = 0; i < 8; i++)
#pragma unroll
            for (int e = 0; e < 4; e++) bad |= d[i][e] ^ tag;
          if (__builtin_amdgcn_ballot_w64((bad & 0xff) != 0) == 0) break;
          FENCE();
        }
        ST(4);
#pragma unroll
        for (int h2 = 0; h2 < 2; h2++) {
          float v[4];
#pragma unroll
          for (int e = 0; e < 4; e++) v[e] = (asf(d[h2][e] ^ tag) + asf(d[2 + h2][e] ^ tag)) + (asf(d[4 + h2][e] ^ tag) + asf(d[6 + h2][e] ^ tag));
          *(LAS v2u*)(S + L_Q + (2 * h2 + (lane >> 5)) * 320 + (lane & 31) * 8) = v2u{pk(v[0], v[1]), pk(v[2], v[3])};
        }
        post(F_Q);
        ST(5);
      } else {
        const u32 off = wline(W_HF + (s * 2 + (x & 1)) * 8) + cp + lane * 16;
        v4u d;
_Pragma("unroll 1") for (u32 it = 0; it < SPIN_CAP; it++) {
          d = __builtin_amdgcn_raw_buffer_load_b128(rw, off, 0, 16);
          const u32 bad = (d[0] ^ tag) | (d[1] ^ tag) | (d[2] ^ tag) | (d[3] ^ tag);
          if (__builtin_amdgcn_ballot_w64((bad & 0xff) != 0) == 0) break;
          FENCE();
        }
        ST(4);
        const float a0 = sum32(asf(d[0] ^ tag), asf(d[2] ^ tag)), a1 = sum32(asf(d[1] ^ tag), asf(d[3] ^ tag));
        float h = sum16(a0, a1);
        h += dpp<ROR8>(h);
        const u32 idx = 4 * (lane & 7) + kg;
        h += asf((u32)(*(LAS unsigned short*)(S + L_DUMMY + idx * 2)) << 16);
        const float e = __builtin_amdgcn_exp2f(-1.4426950408889634f * fabsf(h));
        const float num = h > 0.f ? 1.f + 2.f * e : e * e + 2.f * e;
        const float den = num + (h > 0.f ? 2.f * e * e : 2.f);
        const float act = h * num * __builtin_amdgcn_rcpf(den);
        *(LAS unsigned short*)(S + L_ACT + idx * 2) = (unsigned short)pk(act, act);
        post(F_ACT);
        ST(5);
      }
    } else if (wv == 3) {
      wait(F_LSE, 1);
      ST(2);
      const u32 loff = wline(W_LSE + gc * 32 + (lane & 31)) + cp + (2 * (hh & 1) + (lane >> 5)) * 8;
      v2u z;
_Pragma("unroll 1") for (u32 it = 0; it < SPIN_CAP; it++) {
        z = __builtin_amdgcn_raw_buffer_load_b64(rw, loff, 0, 16);
        if (__builtin_amdgcn_ballot_w64(z[1] != ep) == 0) break;
        FENCE();
      }
      ST(3);
      const float lse = asf(z[0]);
      float M = fmaxf(lse, dpp<XOR1>(lse)); M = fmaxf(M, dpp<XOR2>(M)); M = fmaxf(M, dpp<ROR4>(M)); M = fmaxf(M, dpp<ROR8>(M)); M = max16(M);
      const float e = __builtin_amdgcn_exp2f(lse - M);
      float t = e + dpp<XOR1>(e); t += dpp<XOR2>(t); t += dpp<ROR4>(t); t += dpp<ROR8>(t); t = sum16(t, t);
      const float wt = e * __builtin_amdgcn_rcpf(t);
      *(LAS unsigned short*)(S + L_WT + lane * 2) = (unsigned short)pk(wt, wt);
      FENCE();
      const v4u wr = *(LAS v4u*)(S + L_WT + (q4 & 1) * 64 + kg * 16);
      const u32 lo = (p4 & 1) ? 0x01000c0cu : 0x0c0c0100u, hi = (p4 & 1) ? 0x03020c0cu : 0x0c0c0302u, zz = 0x0c0c0c0cu;
      const u32 s0 = (p4 & 2) ? zz : lo, s1 = (p4 & 2) ? lo : zz, s2 = (p4 & 2) ? zz : hi, s3 = (p4 & 2) ? hi : zz;
      v4u A[4];
#pragma unroll
      for (int m = 0; m < 4; m++) A[m] = v4u{__builtin_amdgcn_perm(0, wr[m], s0), __builtin_amdgcn_perm(0, wr[m], s1), __builtin_amdgcn_perm(0, wr[m], s2), __builtin_amdgcn_perm(0, wr[m], s3)};
      wait(F_OPUB, 1);
      ST(4);
      const u32 ooff = wline(W_O + (gc * 8 + hh) * 32) + cp + kg * 1024 + col * 8;
      v2u d[8];
_Pragma("unroll 1") for (u32 it = 0; it < SPIN_CAP; it++) {
        u32 mx = 0;
#pragma unroll
        for (int i = 0; i < 8; i++) d[i] = __builtin_amdgcn_raw_buffer_load_b64(rw, ooff + i * 128, 0, 16);
#pragma unroll
        for (int i = 0; i < 8; i++) mx = max(mx, max(d[i][0], d[i][1]));
        if (__builtin_amdgcn_ballot_w64(mx == ~0u) == 0) break;
        FENCE();
      }
      ST(5);
      v4f o = {};
#pragma unroll
      for (int m = 0; m < 4; m++) o = mfma(A[m], v4u{d[2 * m][0], d[2 * m][1], d[2 * m + 1][0], d[2 * m + 1][1]}, o);
      if (kg == (col >> 3)) *(LAS v2u*)(S + L_O + col * 8) = v2u{pk(o[0], o[1]), pk(o[2], o[3])};
      post(F_O);
      ST(6);
    } else {
      const u32 w = wv - 4;
      wait(F_PROG, M_K + 8 * (w + 1));
      v4u kf[2][4];
      {
        LAS char* kb = S + L_K + (8 * w + q4) * PB + p4 * 256;
#pragma unroll
        for (int ks = 0; ks < 4; ks++)
#pragma unroll
          for (int t = 0; t < 2; t++) kf[t][ks] = *(LAS v4u*)(kb + t * 4 * PB + (((4 * ks) ^ (4 * p4)) + kg) * 16);
      }
      ST(2);
      wait(F_Q, 1);
      v4f sc[2] = {};
#pragma unroll
      for (int ks = 0; ks < 4; ks++) {
        const v4u qf = *(LAS v4u*)(S + L_Q + p4 * 320 + ks * 64 + kg * 16);
        sc[0] = mfma(kf[0][ks], qf, sc[0]); sc[1] = mfma(kf[1][ks], qf, sc[1]);
      }
      constexpr float C2 = 0.08838834764831845f * 1.4426950408889634f;
      float m = fmaxf(fmaxf(fmaxf(sc[0][0], sc[0][1]), fmaxf(sc[0][2], sc[0][3])), fmaxf(fmaxf(sc[1][0], sc[1][1]), fmaxf(sc[1][2], sc[1][3])));
      m = max32(max16(m)) * C2;
      float pe[8], l = 0.f;
#pragma unroll
      for (int j = 0; j < 8; j++) { pe[j] = __builtin_amdgcn_exp2f(fmaf(sc[j >> 2][j & 3], C2, -m)); l += pe[j]; }
      l = sum16(l, l); l = sum32(l, l);
      *(LAS v4u*)(S + L_P + w * 1024 + lane * 16) = v4u{pk(pe[0], pe[1]), pk(pe[2], pe[3]), pk(pe[4], pe[5]), pk(pe[6], pe[7])};
      if (lane < 4) *(LAS v2f*)(S + L_ML + lane * 32 + w * 8) = v2f{m, l};
      asm volatile("s_waitcnt vmcnt(0)" ::: "memory");
      post(F_P0 + w);
      ST(3);
      while ((flag[F_P0] & flag[F_P0 + 1] & flag[F_P0 + 2] & flag[F_P0 + 3]) == 0) { }
      FENCE();
      const v4f ml0 = *(LAS v4f*)(S + L_ML + p4 * 32), ml1 = *(LAS v4f*)(S + L_ML + p4 * 32 + 16);
      v4u pf[4];
#pragma unroll
      for (int ks = 0; ks < 4; ks++) pf[ks] = *(LAS v4u*)(S + L_P + ks * 1024 + lane * 16);
      const float M = fmaxf(fmaxf(ml0[0], ml0[2]), fmaxf(ml1[0], ml1[2]));
      float f[4] = {__builtin_amdgcn_exp2f(ml0[0] - M), __builtin_amdgcn_exp2f(ml0[2] - M), __builtin_amdgcn_exp2f(ml1[0] - M), __builtin_amdgcn_exp2f(ml1[2] - M)};
      const float L = (f[0] * ml0[1] + f[1] * ml0[3]) + (f[2] * ml1[1] + f[3] * ml1[3]);
      if (w == 0) {
        const float lse = M + __builtin_amdgcn_logf(L);
        if (lane < 32) __builtin_amdgcn_raw_buffer_store_b64(v2u{asu(lse), ep}, rw, wline(W_LSE + x * 32 + s) + kg * 8192 + col * 8, 0, 16);
        post(F_LSE);
      }
      const float rl = __builtin_amdgcn_rcpf(L);
      const u32 tro = kg * PB + q4 * 256 + 8 * (p4 & 1);
      const u32 t0 = tro + (((4 * w) ^ (4 * q4)) + (p4 >> 1)) * 16;
      auto frag = [&](int base, int ks, int u) {
        LAS char* pa = S + base + t0 + u * 32 + ks * 8 * PB;
        const v2u a0 = __builtin_bit_cast(v2u, __builtin_amdgcn_ds_read_tr16_b64_v4i16((LAS v4s*)(pa)));
        const v2u a1 = __builtin_bit_cast(v2u, __builtin_amdgcn_ds_read_tr16_b64_v4i16((LAS v4s*)(pa + 4 * PB)));
        return v4u{a0[0], a0[1], a1[0], a1[1]};
      };
      ST(4);
      v4f acc = {};
#pragma unroll
      for (int ks = 0; ks < 4; ks++) {
        wait(F_PROG, M_V + 8 * (ks + 1));
        const v4f d0 = mfma(frag(L_V, ks, 0), pf[ks], v4f{}), d1 = mfma(frag(L_V, ks, 1), pf[ks], v4f{});
        const float fk = f[ks] * rl;
#pragma unroll
        for (int r = 0; r < 4; r++) acc[r] = fmaf(fk, (q4 & 1) ? d1[r] : d0[r], acc[r]);
      }
      __builtin_amdgcn_raw_buffer_store_b64(v2u{min(pk(acc[0], acc[1]), ~1u), min(pk(acc[2], acc[3]), ~1u)}, rw, o_off, 0, 16);
      post(F_OPUB);
      ST(5);
      wait(F_PROG, M_ALL);
      v4u wf[2][2], w2f[2];
#pragma unroll
      for (int u = 0; u < 2; u++) { wf[0][u] = frag(L_WO, 0, u); wf[1][u] = frag(L_WO, 1, u); w2f[u] = frag(L_W2, 0, u); }
      ST(6);
      wait(F_ACT, 1);
      const float g = *(LAS float*)(S + L_FLAG + 4 * F_G);
      v4f pre = {};
      if (ffn) {
        const v2u a0 = *(LAS v2u*)(S + L_ACT + kg * 8), a1 = *(LAS v2u*)(S + L_ACT + kg * 8 + 32);
        const v4u ba = {a0[0], a0[1], a1[0], a1[1]};
        const v4f b0 = mfma(w2f[0], ba, v4f{}), b1 = mfma(w2f[1], ba, v4f{});
#pragma unroll
        for (int r = 0; r < 4; r++) pre[r] = (1.f - g) * ((col & 1) ? b1[r] : b0[r]);
      }
      const u32 h3off = wline(W_H3 + (c * 4 + w) * 64 + (x & 1) * 32 + s) + (col >> 1) * 8192 + (col & 1) * 64 + kg * 16;
      ST(7);
      wait(F_O, 1);
      v4f da[2] = {};
#pragma unroll
      for (int ks = 0; ks < 2; ks++) {
        const v2u a0 = *(LAS v2u*)(S + L_O + ks * 64 + kg * 8), a1 = *(LAS v2u*)(S + L_O + ks * 64 + kg * 8 + 32);
        const v4u bo = {a0[0], a0[1], a1[0], a1[1]};
        da[0] = mfma(wf[ks][0], bo, da[0]); da[1] = mfma(wf[ks][1], bo, da[1]);
      }
      v4u o3;
#pragma unroll
      for (int r = 0; r < 4; r++) o3[r] = (asu(fmaf(g, (col & 1) ? da[1][r] : da[0][r], pre[r])) & ~0xffu) | tag;
      if (col < 4) __builtin_amdgcn_raw_buffer_store_b128(o3, rw, h3off, 0, 16);
      ST(8);
      if (s >= 24 && w == 0) {
        const u32 idx = (x & 1) * 8 + (s - 24), cc = c ^ 1;
        const u32 off = wline(W_H3 + (cc * 4 + (idx >> 2)) * 64) + cp + (lane >> 1) * 128 + (idx & 3) * 32 + (lane & 1) * 16;
        v4u d0, d1;
_Pragma("unroll 1") for (u32 it = 0; it < SPIN_CAP; it++) {
          d0 = __builtin_amdgcn_raw_buffer_load_b128(rw, off, 0, 16);
          d1 = __builtin_amdgcn_raw_buffer_load_b128(rw, off, 4096, 16);
          u32 bad = 0;
#pragma unroll
          for (int e = 0; e < 4; e++) bad |= (d0[e] ^ tag) | (d1[e] ^ tag);
          if (__builtin_amdgcn_ballot_w64((bad & 0xff) != 0) == 0) break;
          FENCE();
        }
        ST(9);
        float v[4];
#pragma unroll
        for (int e = 0; e < 4; e++) v[e] = asf(d0[e] ^ tag) + asf(d1[e] ^ tag);
        float y = sum16(sum32(v[0], v[2]), sum32(v[1], v[3]));
        y += dpp<ROR8>(y); y += dpp<ROR4>(y); y += dpp<ROR2>(y);
        if (col < 2) ((unsigned short*)gout)[cc * 128 + idx * 8 + col * 4 + kg] = (unsigned short)pk(y, y);
        if (blockIdx.x == 255 && lane == 0) __builtin_amdgcn_raw_buffer_store_b32(ep, rw, 0, 0, 16);
        ST(10);
      }
    }
  }
done:;
}

extern "C" void kernel_launch(void* const* d_in, const int* in_sizes, int n_in, void* d_out, int out_size, void* d_ws, size_t ws_size, hipStream_t stream) {
  fused<<<256, 512, 0, stream>>>(d_in[0], d_in[1], d_in[2], d_in[3], d_in[4], d_in[6], d_in[7], d_in[8], d_in[9], d_out, d_ws);
}
```

```cpp
#define DIAG 2
#include <hip/hip_runtime.h>
#include <hip/hip_bf16.h>

#define DEPTH 24
#define QX 0
#define QPOLL_AT 40
#define POL 2
#define STAMPS 0

#define LAS __attribute__((address_space(3)))
typedef unsigned u32;
typedef unsigned long long u64;
typedef u32 v4u __attribute__((ext_vector_type(4)));
typedef u32 v2u __attribute__((ext_vector_type(2)));
typedef float v4f __attribute__((ext_vector_type(4)));
typedef float v2f __attribute__((ext_vector_type(2)));
typedef short v4s __attribute__((ext_vector_type(4)));
typedef __bf16 v8b __attribute__((ext_vector_type(8)));
typedef __bf16 v2b __attribute__((ext_vector_type(2)));
typedef __amdgpu_buffer_rsrc_t rsrc_t;

constexpr int PB = 1056;
constexpr int L_X = 0, L_GW = 1024, L_MISC = 2048, L_DUMMY = 3072;
constexpr int L_WQ = 4096, L_K = L_WQ + 16384, L_V = L_K + 32 * PB, L_W1 = L_V + 32 * PB, L_WO = L_W1 + 8192, L_W2 = L_WO + 16 * PB;
constexpr int L_Q = L_W2 + 8 * PB, L_P = L_Q + 1280, L_ML = L_P + 4096, L_WT = L_ML + 128, L_O = L_WT + 128, L_ACT = L_O + 128, L_FLAG = L_ACT + 64;
constexpr int L_END = L_FLAG + 64;
enum { F_PROG = 0, F_Q = 1, F_LSE = 2, F_OPUB = 3, F_P0 = 4, F_ACT = 8, F_O = 9, F_G = 10 };
constexpr int W_H1 = 64, W_LSE = 576, W_O = 832, W_HF = 2880, W_H3 = 3328, W_LINES = 4352;
constexpr int WS_BYTES = (W_LINES / 64) * 16384;
__device__ __forceinline__ u32 wline(u32 L) { return (L >> 6) * 16384 + (L & 63) * 128; }
constexpr int M_WQ = 20, M_K = 20, M_V = 52, M_W1 = 92, M_ALL = 127;
constexpr u32 SPIN_CAP = 1u << 18;

__device__ __forceinline__ rsrc_t mk(const void* p, u32 bytes) { return __builtin_amdgcn_make_buffer_rsrc((void*)p, 0, bytes, 0x00020000); }
__device__ __forceinline__ v4f mfma(v4u a, v4u b, v4f c) { return __builtin_amdgcn_mfma_f32_16x16x32_bf16(__builtin_bit_cast(v8b, a), __builtin_bit_cast(v8b, b), c, 0, 0, 0); }
__device__ __forceinline__ u32 pk(float lo, float hi) { v2f f = {lo, hi}; return __builtin_bit_cast(u32, __builtin_convertvector(f, v2b)); }
__device__ __forceinline__ float asf(u32 x) { return __builtin_bit_cast(float, x); }
__device__ __forceinline__ u32 asu(float x) { return __builtin_bit_cast(u32, x); }
template <int C> __device__ __forceinline__ float dpp(float x) { return asf(__builtin_amdgcn_mov_dpp(asu(x), C, 0xf, 0xf, true)); }
constexpr int XOR1 = 0xB1, XOR2 = 0x4E, XOR7 = 0x141, ROR8 = 0x128, ROR4 = 0x124, ROR2 = 0x122;
__device__ __forceinline__ float sum16(float a, float b) { auto r = __builtin_amdgcn_permlane16_swap(asu(a), asu(b), false, false); return asf(r[0]) + asf(r[1]); }
__device__ __forceinline__ float sum32(float a, float b) { auto r = __builtin_amdgcn_permlane32_swap(asu(a), asu(b), false, false); return asf(r[0]) + asf(r[1]); }
__device__ __forceinline__ float max16(float a) { auto r = __builtin_amdgcn_permlane16_swap(asu(a), asu(a), false, false); return fmaxf(asf(r[0]), asf(r[1])); }
__device__ __forceinline__ float max32(float a) { auto r = __builtin_amdgcn_permlane32_swap(asu(a), asu(a), false, false); return fmaxf(asf(r[0]), asf(r[1])); }
__device__ __forceinline__ float dot2(u32 a, u32 b, float c) { return __builtin_amdgcn_fdot2_f32_bf16(__builtin_bit_cast(v2b, a), __builtin_bit_cast(v2b, b), c, false); }
#define FENCE() asm volatile("" ::: "memory")

#define ST(i) do {} while (0)

template <int NT> __device__ __forceinline__ void dma(u32 lds, u32 voff, rsrc_t r, u32 soff) {
  if (NT == 1) asm volatile("s_mov_b32 m0, %0\n\ts_nop 0\n\tbuffer_load_dwordx4 %1, %2, %3 offen nt lds" ::"s"(lds), "v"(voff), "s"(r), "s"(soff) : "memory");
  else if (NT == 2) asm volatile("s_mov_b32 m0, %0\n\ts_nop 0\n\tbuffer_load_dwordx4 %1, %2, %3 offen sc1 lds" ::"s"(lds), "v"(voff), "s"(r), "s"(soff) : "memory");
  else asm volatile("s_mov_b32 m0, %0\n\ts_nop 0\n\tbuffer_load_dwordx4 %1, %2, %3 offen lds" ::"s"(lds), "v"(voff), "s"(r), "s"(soff) : "memory");
}
template <int LP, int SP> __device__ __forceinline__ void dma4(u32 lds, u32 voff, rsrc_t r, u32 soff) {
  asm volatile("s_mov_b32 m0, %1\n\ts_nop 0\n\tbuffer_load_dwordx4 %2, %3, %0 offen nt lds\n\t"
               "s_add_u32 m0, m0, %4\n\ts_add_u32 %0, %0, %5\n\tbuffer_load_dwordx4 %2, %3, %0 offen nt lds\n\t"
               "s_add_u32 m0, m0, %4\n\ts_add_u32 %0, %0, %5\n\tbuffer_load_dwordx4 %2, %3, %0 offen nt lds\n\t"
               "s_add_u32 m0, m0, %4\n\ts_add_u32 %0, %0, %5\n\tbuffer_load_dwordx4 %2, %3, %0 offen nt lds"
               : "+s"(soff) : "s"(lds), "v"(voff), "s"(r), "n"(LP), "n"(SP) : "memory", "scc");
}
template <int N> __device__ __forceinline__ void prog(u32 flag_addr, int v) {
  asm volatile("s_waitcnt vmcnt(%2)\n\tds_write_b32 %0, %1" ::"v"(flag_addr), "v"(v), "n"(N) : "memory");
}
template <int K> __device__ __forceinline__ void drain(u32 fa, int n) {
  if constexpr (K > 0) { prog<K>(fa, n - K); drain<K - 4>(fa, n); }
}

__global__ __launch_bounds__(512, 2) void fused(const void* __restrict__ gx, const void* __restrict__ gK, const void* __restrict__ gV,
                                                const void* __restrict__ gwq, const void* __restrict__ gwo, const void* __restrict__ gw1,
                                                const void* __restrict__ gb1, const void* __restrict__ gw2, const void* __restrict__ ggw,
                                                void* __restrict__ gout, void* __restrict__ gws) {
  __shared__ __attribute__((aligned(16))) char smem[L_END];
  LAS char* const S = (LAS char*)smem;
  const u32 sb = (u32)(size_t)S;
  volatile LAS int* const flag = (volatile LAS int*)(S + L_FLAG);
  const u32 x = blockIdx.x & 7, s = blockIdx.x >> 3;
  const u32 lane = threadIdx.x & 63, wv = __builtin_amdgcn_readfirstlane(threadIdx.x >> 6);
  const bool ffn = s < 28;
  const u32 c = x >> 1, gc = 2 * (s >> 3) + ((x & 1) ^ 1), hh = s & 7;
  u64 dg0, dgc0, dgk, dg1 = 0, dg2 = 0;
  asm volatile("s_memrealtime %0\n\ts_memtime %1" : "=s"(dg0), "=s"(dgc0) :: "memory");
  __builtin_amdgcn_sched_barrier(0);
  asm volatile("s_waitcnt lgkmcnt(0)\n\ts_memrealtime %0\n\ts_waitcnt lgkmcnt(0)" : "=s"(dgk) : "s"(gws) : "memory");
  if (threadIdx.x == 256) __hip_atomic_store((u64*)((char*)gws + (2 << 20)) + blockIdx.x * 16, dg0, __ATOMIC_RELAXED, __HIP_MEMORY_SCOPE_AGENT);
#define RT(v) asm volatile("s_memrealtime %0\n\ts_waitcnt lgkmcnt(0)" : "=s"(v) :: "memory")
  auto wait = [&](int f, int v) { while (flag[f] < v) { __builtin_amdgcn_s_sleep(1); } FENCE(); };
  auto post = [&](int f) { FENCE(); flag[f] = 1; };

  if (__builtin_expect(wv == 0, 1)) {
    __builtin_amdgcn_s_setprio(3);
    const u32 fa = sb + L_FLAG;
    const u32 v16 = lane * 16;
    dma<0>(sb + L_X, v16, mk(gx, 1024), 0);
    const u32 r8 = lane >> 3, sw8 = ((lane & 7) ^ (r8 & 6)) * 16;
    const u32 r4 = lane >> 4, sw4 = ((lane & 15) ^ (4 * r4)) * 16;
    const rsrc_t rq = mk(gwq, 512 * 4096 * 2);
    const u32 vq = r8 * 8192 + sw8;
    u32 so = (s >> 3) * (128 * 8192) + (x ^ QX) * 1024 + (s & 7) * 128, la = sb + L_WQ;
#pragma unroll 1
    for (int i = 0; i < 4; i++) { dma4<1024, 8 * 8192>(la, vq, rq, so); la += 4096; so += 32 * 8192; }
    dma<2>(sb + L_MISC, v16, mk(gws, 128), 0);
    dma<0>(sb + L_GW, v16, mk(ggw, 1024), 0);
    dma<0>(sb + L_DUMMY, v16, mk(gb1, 3584), s * 128 + (x & 1) * 64);
    ST(1);
    __builtin_amdgcn_s_barrier();
    int n = 20;
    const u32 vk = r4 * 256 + sw4;
    const u32 kv0 = (x * 65536 + 61440 + s * 128) * 256;
    la = sb + L_K;
#pragma unroll 1
    for (int t = 0; t < 2; t++) {
      const rsrc_t r = mk(t ? gV : gK, 8u * 65536 * 256);
#pragma unroll 1
      for (int i = 0; i < 32; i += 4) { dma4<PB, 1024>(la, vk, r, kv0 + ((i ^ ((s & 3) << 3)) << 10)); la += 4 * PB; n += 4; prog<DEPTH>(fa, n - DEPTH); }
    }
    ST(2);
    if (ffn) {
      const rsrc_t r1 = mk(gw1, 512 * 1792 * 2);
      const u32 v1 = r8 * 3584 + sw8;
      so = x * (64 * 3584) + s * 128;
#pragma unroll 1
      for (int i = 0; i < 2; i++) { dma4<1024, 8 * 3584>(la, v1, r1, so); la += 4096; so += 32 * 3584; n += 4; prog<DEPTH>(fa, n - DEPTH); }
    }
    const u32 vo = r4 * 1024 + sw4;
    const rsrc_t ro = mk(gwo, 4096 * 512 * 2);
    la = sb + L_WO;
    so = (gc * 512 + (hh & 1) * 256 + (hh >> 1) * 32) * 1024 + c * 256;
#pragma unroll 1
    for (int i = 0; i < 4; i++) { dma4<PB, 4096>(la, vo, ro, so + (i & 1) * 16384 + (i >> 1) * (128 * 1024)); la += 4 * PB; n += 4; prog<DEPTH>(fa, n - DEPTH); }
    if (ffn) {
      const rsrc_t r2 = mk(gw2, 1792 * 512 * 2);
      so = (s * 64 + (x & 1) * 32) * 1024 + c * 256;
#pragma unroll 1
      for (int i = 0; i < 2; i++) { dma4<PB, 4096>(la, vo, r2, so); la += 4 * PB; so += 16384; n += 4; prog<DEPTH>(fa, n - DEPTH); }
    }
    ST(3);
    drain<DEPTH - 4>(fa, n);
    prog<0>(fa, M_ALL);
    ST(4);
  } else {
    const rsrc_t rw = mk(gws, WS_BYTES);
    const u32 col = lane & 15, kg = lane >> 4, q4 = col >> 2, p4 = col & 3;
    const u32 cp = (POL == 2) ? 0 : ((((x >> 2) & 1) ^ POL) * 8192);
    if (wv == 3 && lane < 16) flag[lane] = 0;
    u32 o_off = 0;
    if (wv >= 4) {
      const u32 w = wv - 4;
      o_off = wline(W_O + (x * 8 + 2 * w + (p4 >> 1)) * 32 + s) + (col >> 3) * 8192 + (col & 1) * 64 + (q4 & 1) * 32 + kg * 8;
      v2u ones = {~0u, ~0u};
      __builtin_amdgcn_raw_buffer_store_b64(ones, rw, o_off, 0, 16);
    }
    asm volatile("s_waitcnt lgkmcnt(0)" ::: "memory");
    __builtin_amdgcn_s_barrier();
    FENCE();
    wait(F_PROG, M_WQ);
    const u32 ep = __builtin_amdgcn_readfirstlane(*(LAS u32*)(S + L_MISC)) + 1;
    const u32 tag = ep & 0xff;
    RT(dg1);
    ST(1);
    if (wv <= 2) {
      const bool isq = wv == 1;
      if (!isq) {
        const v4u a = *(LAS v4u*)(S + L_X + lane * 16), b = *(LAS v4u*)(S + L_GW + lane * 16);
        float d = 0.f;
#pragma unroll
        for (int i = 0; i < 4; i++) d = dot2(a[i], b[i], d);
        d += dpp<XOR1>(d); d += dpp<XOR2>(d); d += dpp<ROR4>(d); d += dpp<ROR8>(d); d = sum16(d, d); d = sum32(d, d);
        const float g = __builtin_amdgcn_rcpf(1.f + __builtin_amdgcn_exp2f(-1.4426950408889634f * d));
        *(LAS float*)(S + L_FLAG + 4 * F_G) = g;
        if (!ffn) { post(F_ACT); goto done; }
        wait(F_PROG, M_W1);
      }
      {
        const u32 rr = 4 * kg + q4, c6 = rr & 6;
        LAS char* wb = S + (isq ? L_WQ : L_W1) + rr * 128 + 8 * (p4 & 1) + (p4 >> 1) * 16;
        LAS char* xb = S + L_X + (isq ? (s >> 3) * 256 : x * 128) + kg * 8;
        v4f acc[4] = {};
        const int nks = isq ? 4 : 2;
#pragma unroll 1
        for (int ks = 0; ks < nks; ks++) {
          const v2u x0 = *(LAS v2u*)(xb), x1 = *(LAS v2u*)(xb + 32);
          const v4u bx = {x0[0], x0[1], x1[0], x1[1]};
#pragma unroll
          for (int u = 0; u < 4; u++) {
            LAS char* pa = wb + ((2 * u) ^ c6) * 16;
            const v2u a0 = __builtin_bit_cast(v2u, __builtin_amdgcn_ds_read_tr16_b64_v4i16((LAS v4s*)(pa)));
            const v2u a1 = __builtin_bit_cast(v2u, __builtin_amdgcn_ds_read_tr16_b64_v4i16((LAS v4s*)(pa + 2048)));
            acc[u] = mfma(v4u{a0[0], a0[1], a1[0], a1[1]}, bx, acc[u]);
          }
          wb += 4096; xb += 64;
        }
        v4u o;
#pragma unroll
        for (int r = 0; r < 4; r++) {
          const float v = (p4 & 2) ? ((p4 & 1) ? acc[3][r] : acc[2][r]) : ((p4 & 1) ? acc[1][r] : acc[0][r]);
          o[r] = (asu(v) & ~0xffu) | tag;
        }
        const u32 L = isq ? W_H1 + (x ^ QX) * 64 + s * 2 + (p4 >> 1) : W_HF + (s * 2 + (p4 >> 1)) * 8 + x;
        if (col < 8) __builtin_amdgcn_raw_buffer_store_b128(o, rw, wline(L) + q4 * 8192 + (p4 & 1) * 64 + kg * 16, 0, 16);
      }
      ST(2);
      if (isq) {
        wait(F_PROG, QPOLL_AT);
        ST(3);
        const u32 off = wline(W_H1 + x * 64) + cp + lane * 16;
        v4u d[8];
_Pragma("unroll 1") for (u32 it = 0; it < SPIN_CAP; it++) {
          u32 bad = 0;
#pragma unroll
          for (int i = 0; i < 8; i++) d[i] = __builtin_amdgcn_raw_buffer_load_b128(rw, off, i * 1024, 16);
#pragma unroll
          for (int i = 0; i < 8; i++)
#pragma unroll
            for (int e = 0; e < 4; e++) bad |= d[i][e] ^ tag;
          if (__builtin_amdgcn_ballot_w64((bad & 0xff) != 0) == 0) break;
          FENCE();
        }
        ST(4);
#pragma unroll
        for (int h2 = 0; h2 < 2; h2++) {
          float v[4];
#pragma unroll
          for (int e = 0; e < 4; e++) v[e] = (asf(d[h2][e] ^ tag) + asf(d[2 + h2][e] ^ tag)) + (asf(d[4 + h2][e] ^ tag) + asf(d[6 + h2][e] ^ tag));
          *(LAS v2u*)(S + L_Q + (2 * h2 + (lane >> 5)) * 320 + (lane & 31) * 8) = v2u{pk(v[0], v[1]), pk(v[2], v[3])};
        }
        post(F_Q);
        ST(5);
      } else {
        const u32 off = wline(W_HF + (s * 2 + (x & 1)) * 8) + cp + lane * 16;
        v4u d;
_Pragma("unroll 1") for (u32 it = 0; it < SPIN_CAP; it++) {
          d = __builtin_amdgcn_raw_buffer_load_b128(rw, off, 0, 16);
          const u32 bad = (d[0] ^ tag) | (d[1] ^ tag) | (d[2] ^ tag) | (d[3] ^ tag);
          if (__builtin_amdgcn_ballot_w64((bad & 0xff) != 0) == 0) break;
          FENCE();
        }
        ST(4);
        const float a0 = sum32(asf(d[0] ^ tag), asf(d[2] ^ tag)), a1 = sum32(asf(d[1] ^ tag), asf(d[3] ^ tag));
        float h = sum16(a0, a1);
        h += dpp<ROR8>(h);
        const u32 idx = 4 * (lane & 7) + kg;
        h += asf((u32)(*(LAS unsigned short*)(S + L_DUMMY + idx * 2)) << 16);
        const float e = __builtin_amdgcn_exp2f(-1.4426950408889634f * fabsf(h));
        const float num = h > 0.f ? 1.f + 2.f * e : e * e + 2.f * e;
        const float den = num + (h > 0.f ? 2.f * e * e : 2.f);
        const float act = h * num * __builtin_amdgcn_rcpf(den);
        *(LAS unsigned short*)(S + L_ACT + idx * 2) = (unsigned short)pk(act, act);
        post(F_ACT);
        ST(5);
      }
    } else if (wv == 3) {
      wait(F_LSE, 1);
      ST(2);
      const u32 loff = wline(W_LSE + gc * 32 + (lane & 31)) + cp + (2 * (hh & 1) + (lane >> 5)) * 8;
      v2u z;
_Pragma("unroll 1") for (u32 it = 0; it < SPIN_CAP; it++) {
        z = __builtin_amdgcn_raw_buffer_load_b64(rw, loff, 0, 16);
        if (__builtin_amdgcn_ballot_w64(z[1] != ep) == 0) break;
        FENCE();
      }
      ST(3);
      const float lse = asf(z[0]);
      float M = fmaxf(lse, dpp<XOR1>(lse)); M = fmaxf(M, dpp<XOR2>(M)); M = fmaxf(M, dpp<ROR4>(M)); M = fmaxf(M, dpp<ROR8>(M)); M = max16(M);
      const float e = __builtin_amdgcn_exp2f(lse - M);
      float t = e + dpp<XOR1>(e); t += dpp<XOR2>(t); t += dpp<ROR4>(t); t += dpp<ROR8>(t); t = sum16(t, t);
      const float wt = e * __builtin_amdgcn_rcpf(t);
      *(LAS unsigned short*)(S + L_WT + lane * 2) = (unsigned short)pk(wt, wt);
      FENCE();
      const v4u wr = *(LAS v4u*)(S + L_WT + (q4 & 1) * 64 + kg * 16);
      const u32 lo = (p4 & 1) ? 0x01000c0cu : 0x0c0c0100u, hi = (p4 & 1) ? 0x03020c0cu : 0x0c0c0302u, zz = 0x0c0c0c0cu;
      const u32 s0 = (p4 & 2) ? zz : lo, s1 = (p4 & 2) ? lo : zz, s2 = (p4 & 2) ? zz : hi, s3 = (p4 & 2) ? hi : zz;
      v4u A[4];
#pragma unroll
      for (int m = 0; m < 4; m++) A[m] = v4u{__builtin_amdgcn_perm(0, wr[m], s0), __builtin_amdgcn_perm(0, wr[m], s1), __builtin_amdgcn_perm(0, wr[m], s2), __builtin_amdgcn_perm(0, wr[m], s3)};
      wait(F_OPUB, 1);
      ST(4);
      const u32 ooff = wline(W_O + (gc * 8 + hh) * 32) + cp + kg * 1024 + col * 8;
      v2u d[8];
_Pragma("unroll 1") for (u32 it = 0; it < SPIN_CAP; it++) {
        u32 mx = 0;
#pragma unroll
        for (int i = 0; i < 8; i++) d[i] = __builtin_amdgcn_raw_buffer_load_b64(rw, ooff + i * 128, 0, 16);
#pragma unroll
        for (int i = 0; i < 8; i++) mx = max(mx, max(d[i][0], d[i][1]));
        if (__builtin_amdgcn_ballot_w64(mx == ~0u) == 0) break;
        FENCE();
      }
      ST(5);
      v4f o = {};
#pragma unroll
      for (int m = 0; m < 4; m++) o = mfma(A[m], v4u{d[2 * m][0], d[2 * m][1], d[2 * m + 1][0], d[2 * m + 1][1]}, o);
      if (kg == (col >> 3)) *(LAS v2u*)(S + L_O + col * 8) = v2u{pk(o[0], o[1]), pk(o[2], o[3])};
      post(F_O);
      ST(6);
    } else {
      const u32 w = wv - 4;
      wait(F_PROG, M_K + 8 * (w + 1));
      v4u kf[2][4];
      {
        LAS char* kb = S + L_K + (8 * w + q4) * PB + p4 * 256;
#pragma unroll
        for (int ks = 0; ks < 4; ks++)
#pragma unroll
          for (int t = 0; t < 2; t++) kf[t][ks] = *(LAS v4u*)(kb + t * 4 * PB + (((4 * ks) ^ (4 * p4)) + kg) * 16);
      }
      ST(2);
      wait(F_Q, 1);
      v4f sc[2] = {};
#pragma unroll
      for (int ks = 0; ks < 4; ks++) {
        const v4u qf = *(LAS v4u*)(S + L_Q + p4 * 320 + ks * 64 + kg * 16);
        sc[0] = mfma(kf[0][ks], qf, sc[0]); sc[1] = mfma(kf[1][ks], qf, sc[1]);
      }
      constexpr float C2 = 0.08838834764831845f * 1.4426950408889634f;
      float m = fmaxf(fmaxf(fmaxf(sc[0][0], sc[0][1]), fmaxf(sc[0][2], sc[0][3])), fmaxf(fmaxf(sc[1][0], sc[1][1]), fmaxf(sc[1][2], sc[1][3])));
      m = max32(max16(m)) * C2;
      float pe[8], l = 0.f;
#pragma unroll
      for (int j = 0; j < 8; j++) { pe[j] = __builtin_amdgcn_exp2f(fmaf(sc[j >> 2][j & 3], C2, -m)); l += pe[j]; }
      l = sum16(l, l); l = sum32(l, l);
      *(LAS v4u*)(S + L_P + w * 1024 + lane * 16) = v4u{pk(pe[0], pe[1]), pk(pe[2], pe[3]), pk(pe[4], pe[5]), pk(pe[6], pe[7])};
      if (lane < 4) *(LAS v2f*)(S + L_ML + lane * 32 + w * 8) = v2f{m, l};
      asm volatile("s_waitcnt vmcnt(0)" ::: "memory");
      post(F_P0 + w);
      ST(3);
      while ((flag[F_P0] & flag[F_P0 + 1] & flag[F_P0 + 2] & flag[F_P0 + 3]) == 0) { }
      FENCE();
      const v4f ml0 = *(LAS v4f*)(S + L_ML + p4 * 32), ml1 = *(LAS v4f*)(S + L_ML + p4 * 32 + 16);
      v4u pf[4];
#pragma unroll
      for (int ks = 0; ks < 4; ks++) pf[ks] = *(LAS v4u*)(S + L_P + ks * 1024 + lane * 16);
      const float M = fmaxf(fmaxf(ml0[0], ml0[2]), fmaxf(ml1[0], ml1[2]));
      float f[4] = {__builtin_amdgcn_exp2f(ml0[0] - M), __builtin_amdgcn_exp2f(ml0[2] - M), __builtin_amdgcn_exp2f(ml1[0] - M), __builtin_amdgcn_exp2f(ml1[2] - M)};
      const float L = (f[0] * ml0[1] + f[1] * ml0[3]) + (f[2] * ml1[1] + f[3] * ml1[3]);
      if (w == 0) {
        const float lse = M + __builtin_amdgcn_logf(L);
        if (lane < 32) __builtin_amdgcn_raw_buffer_store_b64(v2u{asu(lse), ep}, rw, wline(W_LSE + x * 32 + s) + kg * 8192 + col * 8, 0, 16);
        post(F_LSE);
      }
      const float rl = __builtin_amdgcn_rcpf(L);
      const u32 tro = kg * PB + q4 * 256 + 8 * (p4 & 1);
      const u32 t0 = tro + (((4 * w) ^ (4 * q4)) + (p4 >> 1)) * 16;
      auto frag = [&](int base, int ks, int u) {
        LAS char* pa = S + base + t0 + u * 32 + ks * 8 * PB;
        const v2u a0 = __builtin_bit_cast(v2u, __builtin_amdgcn_ds_read_tr16_b64_v4i16((LAS v4s*)(pa)));
        const v2u a1 = __builtin_bit_cast(v2u, __builtin_amdgcn_ds_read_tr16_b64_v4i16((LAS v4s*)(pa + 4 * PB)));
        return v4u{a0[0], a0[1], a1[0], a1[1]};
      };
      ST(4);
      v4f acc = {};
#pragma unroll
      for (int ks = 0; ks < 4; ks++) {
        wait(F_PROG, M_V + 8 * (ks + 1));
        const v4f d0 = mfma(frag(L_V, ks, 0), pf[ks], v4f{}), d1 = mfma(frag(L_V, ks, 1), pf[ks], v4f{});
        const float fk = f[ks] * rl;
#pragma unroll
        for (int r = 0; r < 4; r++) acc[r] = fmaf(fk, (q4 & 1) ? d1[r] : d0[r], acc[r]);
      }
      __builtin_amdgcn_raw_buffer_store_b64(v2u{min(pk(acc[0], acc[1]), ~1u), min(pk(acc[2], acc[3]), ~1u)}, rw, o_off, 0, 16);
      post(F_OPUB);
      ST(5);
      wait(F_PROG, M_ALL);
      RT(dg2);
      v4u wf[2][2], w2f[2];
#pragma unroll
      for (int u = 0; u < 2; u++) { wf[0][u] = frag(L_WO, 0, u); wf[1][u] = frag(L_WO, 1, u); w2f[u] = frag(L_W2, 0, u); }
      ST(6);
      wait(F_ACT, 1);
      const float g = *(LAS float*)(S + L_FLAG + 4 * F_G);
      v4f pre = {};
      if (ffn) {
        const v2u a0 = *(LAS v2u*)(S + L_ACT + kg * 8), a1 = *(LAS v2u*)(S + L_ACT + kg * 8 + 32);
        const v4u ba = {a0[0], a0[1], a1[0], a1[1]};
        const v4f b0 = mfma(w2f[0], ba, v4f{}), b1 = mfma(w2f[1], ba, v4f{});
#pragma unroll
        for (int r = 0; r < 4; r++) pre[r] = (1.f - g) * ((col & 1) ? b1[r] : b0[r]);
      }
      const u32 h3off = wline(W_H3 + (c * 4 + w) * 64 + (x & 1) * 32 + s) + (col >> 1) * 8192 + (col & 1) * 64 + kg * 16;
      ST(7);
      wait(F_O, 1);
      v4f da[2] = {};
#pragma unroll
      for (int ks = 0; ks < 2; ks++) {
        const v2u a0 = *(LAS v2u*)(S + L_O + ks * 64 + kg * 8), a1 = *(LAS v2u*)(S + L_O + ks * 64 + kg * 8 + 32);
        const v4u bo = {a0[0], a0[1], a1[0], a1[1]};
        da[0] = mfma(wf[ks][0], bo, da[0]); da[1] = mfma(wf[ks][1], bo, da[1]);
      }
      v4u o3;
#pragma unroll
      for (int r = 0; r < 4; r++) o3[r] = (asu(fmaf(g, (col & 1) ? da[1][r] : da[0][r], pre[r])) & ~0xffu) | tag;
      if (col < 4) __builtin_amdgcn_raw_buffer_store_b128(o3, rw, h3off, 0, 16);
      ST(8);
      if (s >= 24 && w == 0) {
        const u32 idx = (x & 1) * 8 + (s - 24), cc = c ^ 1;
        const u32 off = wline(W_H3 + (cc * 4 + (idx >> 2)) * 64) + cp + (lane >> 1) * 128 + (idx & 3) * 32 + (lane & 1) * 16;
        v4u d0, d1;
_Pragma("unroll 1") for (u32 it = 0; it < SPIN_CAP; it++) {
          d0 = __builtin_amdgcn_raw_buffer_load_b128(rw, off, 0, 16);
          d1 = __builtin_amdgcn_raw_buffer_load_b128(rw, off, 4096, 16);
          u32 bad = 0;
#pragma unroll
          for (int e = 0; e < 4; e++) bad |= (d0[e] ^ tag) | (d1[e] ^ tag);
          if (__builtin_amdgcn_ballot_w64((bad & 0xff) != 0) == 0) break;
          FENCE();
        }
        ST(9);
        float v[4];
#pragma unroll
        for (int e = 0; e < 4; e++) v[e] = asf(d0[e] ^ tag) + asf(d1[e] ^ tag);
        float y = sum16(sum32(v[0], v[2]), sum32(v[1], v[3]));
        y += dpp<ROR8>(y); y += dpp<ROR4>(y); y += dpp<ROR2>(y);
        if (col < 2) ((unsigned short*)gout)[cc * 128 + idx * 8 + col * 4 + kg] = (unsigned short)pk(y, y);
        if (blockIdx.x == 255 && lane == 0) __builtin_amdgcn_raw_buffer_store_b32(ep, rw, 0, 0, 16);
        ST(10);
        if (blockIdx.x == 255) {
          u64 now, cnow; asm volatile("s_memrealtime %0\n\ts_memtime %1\n\ts_waitcnt lgkmcnt(0)" : "=s"(now), "=s"(cnow) :: "memory");
          u64 mn = ~0ull, mx = 0;
          for (int i = 0; i < 4; i++) { u64 t = __hip_atomic_load((u64*)((char*)gws + (2 << 20)) + (i * 64 + lane) * 16, __ATOMIC_RELAXED, __HIP_MEMORY_SCOPE_AGENT); mn = t < mn ? t : mn; mx = t > mx ? t : mx; }
          for (int o = 32; o >= 1; o >>= 1) { u64 a = __shfl_xor(mn, o), b = __shfl_xor(mx, o); mn = a < mn ? a : mn; mx = b > mx ? b : mx; }
          u64 X = DIAG == 1 ? now - mn : DIAG == 2 ? mx - mn : DIAG == 3 ? dg1 - dg0 : DIAG == 4 ? dg2 - dg0 : DIAG == 5 ? (u64)((u32)(cnow - dgc0) * 10u / (u32)(now - dg0)) : DIAG == 6 ? dgk - dg0 : 0;
          if (X > 5000) X = 5000;
          const u64 until = now + 10 * X;
          u64 t; do { RT(t); } while (t < until);
        }
      }
    }
  }
done:;
}

extern "C" void kernel_launch(void* const* d_in, const int* in_sizes, int n_in, void* d_out, int out_size, void* d_ws, size_t ws_size, hipStream_t stream) {
  fused<<<256, 512, 0, stream>>>(d_in[0], d_in[1], d_in[2], d_in[3], d_in[4], d_in[6], d_in[7], d_in[8], d_in[9], d_out, d_ws);
}
```

```cpp
#include <hip/hip_runtime.h>
#include <hip/hip_bf16.h>

__global__ __launch_bounds__(512, 2) __attribute__((aligned(4096))) void gated_mix_fused(const void* wq, const void* x, void* ws, const void* w1, const void* k, const void* v,
    const void* gw, const void* b1, const void* wo, const void* w2, void* out) {
#if defined(__HIP_DEVICE_COMPILE__)
  __shared__ __attribute__((aligned(16))) char smem[126464];
  asm volatile(
      "s_load_dwordx2 s[8:9], s[0:1], 0\n"
      "s_load_dwordx2 s[12:13], s[0:1], 8\n"
      "s_load_dwordx2 s[32:33], s[0:1], 16\n"
      "s_load_dwordx2 s[24:25], s[0:1], 80\n"
      "v_readfirstlane_b32 s3, v0\n"
      "v_and_b32 v1, 63, v0\n"
      "s_and_b32 s4, s2, 7\n"
      "s_lshr_b32 s5, s2, 3\n"
      "v_lshrrev_b32 v3, 4, v1\n"
      "v_and_b32 v2, 15, v0\n"
      "v_mov_b32 v6, 0\n"
      "v_bfe_u32 v4, v0, 2, 2\n"
      "v_and_b32 v5, 3, v0\n"
      "v_mov_b32 v8, 1\n"
      "s_lshr_b32 s40, s2, 6\n"
      "s_and_b32 s42, s4, 1\n"
      "s_lshl1_add_u32 s41, s40, s42\n"
      "s_xor_b32 s41, s41, 1\n"
      "s_lshr_b32 s43, s4, 1\n"
      "s_mov_b32 s34, 1441792\n"
      "s_mov_b32 s35, 0x20000\n"
      "s_movk_i32 s14, 1024\n"
      "s_mov_b32 s15, s35\n"
      "s_cmpk_lt_u32 s3, 64\n"
      "s_cbranch_scc1 .Lgm_LQ\n"
      "s_cmpk_eq_u32 s3, 256\n"
      "s_cbranch_scc1 .Lgm_LA\n"
      "s_cmpk_eq_u32 s3, 320\n"
      "s_cbranch_scc1 .Lgm_LTL\n"
      "s_cmpk_eq_u32 s3, 384\n"
      "s_cbranch_scc1 .Lgm_LTF\n"
      "s_cmpk_eq_u32 s3, 448\n"
      "s_cbranch_scc1 .Lgm_LTG\n"
      "v_lshlrev_b32 v10, 3, v1\n"
      "v_mov_b32 v7, 0\n"
      "ds_write_b64 v10, v[6:7]\n"
      "s_waitcnt lgkmcnt(0)\n"
      "s_memrealtime s[84:85]\n"
      "s_cmpk_eq_u32 s3, 64\n"
      "s_cbranch_scc1 .Lgm_LLOAD\n"
      "s_cmpk_eq_u32 s3, 128\n"
      "s_cbranch_scc1 .Lgm_LF\n"
      "s_cmp_eq_u32 s5, 0\n"
      "s_cbranch_scc0 .Lgm_LNOST\n"
      "s_waitcnt lgkmcnt(0)\n"
      "v_lshlrev_b32 v10, 2, v1\n"
      "v_mov_b32 v9, s84\n"
      "s_lshl_b32 s6, s4, 8\n"
      "buffer_store_dword v9, v10, s[32:35], s6 offen offset:1024 sc1\n"
      ".Lgm_LNOST:\n"
      "s_load_dword s36, s[32:33], 0\n"
      "s_branch .Lgm_LG\n"
      ".Lgm_LQ:\n"
      "s_setprio 2\n"
      "v_lshrrev_b32 v9, 3, v1\n"
      "v_and_b32 v7, 7, v0\n"
      "v_bitop3_b32 v7, v9, v7, 6 bitop3:0x6c\n"
      "v_mul_u32_u24 v9, 8192, v9\n"
      "v_lshl_add_u32 v20, v7, 4, v9\n"
      "v_add_u32 v21, 64480, v20\n"
      "v_add_u32 v22, 128960, v20\n"
      "v_add_u32 v23, 193440, v20\n"
      "s_mov_b32 s10, 0x400000\n"
      "s_mov_b32 s11, s35\n"
      "s_lshl_b32 s44, s40, 20\n"
      "s_xor_b32 s6, s4, 1\n"
      "s_lshl_b32 s7, s6, 10\n"
      "s_or_b32 s44, s44, s7\n"
      "s_and_b32 s7, s5, 7\n"
      "s_lshl_b32 s7, s7, 7\n"
      "s_or_b32 s44, s44, s7\n"
      "s_add_u32 s45, s44, 0x40000\n"
      "s_add_u32 s46, s44, 0x80000\n"
      "s_add_u32 s47, s44, 0xc0000\n"
      "s_movk_i32 m0, 8192\n"
      "s_waitcnt lgkmcnt(0)\n"
      "s_memrealtime s[84:85]\n"
      "buffer_load_dwordx4 v20, s[8:11], s44 offen nt lds\n"
      "buffer_load_dwordx4 v21, s[8:11], s44 offen offset:1056 nt lds\n"
      "buffer_load_dwordx4 v22, s[8:11], s44 offen offset:2112 nt lds\n"
      "buffer_load_dwordx4 v23, s[8:11], s44 offen offset:3168 nt lds\n"
      "s_movk_i32 m0, 12416\n"
      "s_nop 0\n"
      "buffer_load_dwordx4 v20, s[8:11], s45 offen nt lds\n"
      "buffer_load_dwordx4 v21, s[8:11], s45 offen offset:1056 nt lds\n"
      "buffer_load_dwordx4 v22, s[8:11], s45 offen offset:2112 nt lds\n"
      "buffer_load_dwordx4 v23, s[8:11], s45 offen offset:3168 nt lds\n"
      "s_movk_i32 m0, 16640\n"
      "s_nop 0\n"
      "buffer_load_dwordx4 v20, s[8:11], s46 offen nt lds\n"
      "buffer_load_dwordx4 v21, s[8:11], s46 offen offset:1056 nt lds\n"
      "buffer_load_dwordx4 v22, s[8:11], s46 offen offset:2112 nt lds\n"
      "buffer_load_dwordx4 v23, s[8:11], s46 offen offset:3168 nt lds\n"
      "s_movk_i32 m0, 20864\n"
      "s_nop 0\n"
      "buffer_load_dwordx4 v20, s[8:11], s47 offen nt lds\n"
      "buffer_load_dwordx4 v21, s[8:11], s47 offen offset:1056 nt lds\n"
      "buffer_load_dwordx4 v22, s[8:11], s47 offen offset:2112 nt lds\n"
      "buffer_load_dwordx4 v23, s[8:11], s47 offen offset:3168 nt lds\n"
      "s_lshl_b32 s7, s40, 8\n"
      "s_add_u32 s46, s12, s7\n"
      "s_addc_u32 s47, s13, 0\n"
      "s_load_dwordx16 s[16:31], s[46:47], 0\n"
      "s_load_dwordx16 s[48:63], s[46:47], 64\n"
      "s_load_dwordx16 s[64:79], s[46:47], 128\n"
      "s_load_dwordx8 s[88:95], s[46:47], 192\n"
      "s_load_dwordx8 s[8:15], s[46:47], 224\n"
      "v_lshl_add_u32 v9, v3, 2, v4\n"
      "v_and_b32 v14, 7, v9\n"
      "v_lshrrev_b32 v15, 3, v9\n"
      "v_mul_u32_u24 v15, 1056, v15\n"
      "v_lshl_add_u32 v15, v14, 7, v15\n"
      "v_lshl_add_u32 v15, v5, 3, v15\n"
      "v_add_u32 v15, 8192, v15\n"
      "v_and_b32 v14, 6, v9\n"
      "v_xor_b32 v9, 0, v14\n"
      "v_lshl_add_u32 v10, v9, 4, v15\n"
      "v_xor_b32 v9, 2, v14\n"
      "v_lshl_add_u32 v11, v9, 4, v15\n"
      "v_xor_b32 v9, 4, v14\n"
      "v_lshl_add_u32 v12, v9, 4, v15\n"
      "v_xor_b32 v9, 6, v14\n"
      "v_lshl_add_u32 v13, v9, 4, v15\n"
      "s_add_u32 s6, s6, 2\n"
      "s_lshl_b32 s6, s6, 13\n"
      "s_lshl_b32 s7, s5, 8\n"
      "s_add_u32 s6, s6, s7\n"
      "s_mov_b32 s7, 0\n"
      "s_addk_i32 s7, 2048\n"
      "v_lshl_add_u32 v15, v3, 3, s7\n"
      "s_barrier\n"
      "s_waitcnt lgkmcnt(0)\n"
      "v_writelane_b32 v9, s16, 0\n"
      "v_writelane_b32 v9, s17, 1\n"
      "v_writelane_b32 v9, s18, 2\n"
      "v_writelane_b32 v9, s19, 3\n"
      "v_writelane_b32 v9, s20, 4\n"
      "v_writelane_b32 v9, s21, 5\n"
      "v_writelane_b32 v9, s22, 6\n"
      "v_writelane_b32 v9, s23, 7\n"
      "v_writelane_b32 v9, s24, 8\n"
      "v_writelane_b32 v9, s25, 9\n"
      "v_writelane_b32 v9, s26, 10\n"
      "v_writelane_b32 v9, s27, 11\n"
      "v_writelane_b32 v9, s28, 12\n"
      "v_writelane_b32 v9, s29, 13\n"
      "v_writelane_b32 v9, s30, 14\n"
      "v_writelane_b32 v9, s31, 15\n"
      "v_writelane_b32 v9, s48, 16\n"
      "v_writelane_b32 v9, s49, 17\n"
      "v_writelane_b32 v9, s50, 18\n"
      "v_writelane_b32 v9, s51, 19\n"
      "v_writelane_b32 v9, s52, 20\n"
      "v_writelane_b32 v9, s53, 21\n"
      "v_writelane_b32 v9, s54, 22\n"
      "v_writelane_b32 v9, s55, 23\n"
      "v_writelane_b32 v9, s56, 24\n"
      "v_writelane_b32 v9, s57, 25\n"
      "v_writelane_b32 v9, s58, 26\n"
      "v_writelane_b32 v9, s59, 27\n"
      "v_writelane_b32 v9, s60, 28\n"
      "v_writelane_b32 v9, s61, 29\n"
      "v_writelane_b32 v9, s62, 30\n"
      "v_writelane_b32 v9, s63, 31\n"
      "v_writelane_b32 v9, s64, 32\n"
      "v_writelane_b32 v9, s65, 33\n"
      "v_writelane_b32 v9, s66, 34\n"
      "v_writelane_b32 v9, s67, 35\n"
      "v_writelane_b32 v9, s68, 36\n"
      "v_writelane_b32 v9, s69, 37\n"
      "v_writelane_b32 v9, s70, 38\n"
      "v_writelane_b32 v9, s71, 39\n"
      "v_writelane_b32 v9, s72, 40\n"
      "v_writelane_b32 v9, s73, 41\n"
      "v_writelane_b32 v9, s74, 42\n"
      "v_writelane_b32 v9, s75, 43\n"
      "v_writelane_b32 v9, s76, 44\n"
      "v_writelane_b32 v9, s77, 45\n"
      "v_writelane_b32 v9, s78, 46\n"
      "v_writelane_b32 v9, s79, 47\n"
      "v_writelane_b32 v9, s88, 48\n"
      "v_writelane_b32 v9, s89, 49\n"
      "v_writelane_b32 v9, s90, 50\n"
      "v_writelane_b32 v9, s91, 51\n"
      "v_writelane_b32 v9, s92, 52\n"
      "v_writelane_b32 v9, s93, 53\n"
      "v_writelane_b32 v9, s94, 54\n"
      "v_writelane_b32 v9, s95, 55\n"
      "v_writelane_b32 v9, s8, 56\n"
      "v_writelane_b32 v9, s9, 57\n"
      "v_writelane_b32 v9, s10, 58\n"
      "v_writelane_b32 v9, s11, 59\n"
      "v_writelane_b32 v9, s12, 60\n"
      "v_writelane_b32 v9, s13, 61\n"
      "v_writelane_b32 v9, s14, 62\n"
      "v_writelane_b32 v9, s15, 63\n"
      "v_lshlrev_b32 v7, 2, v1\n"
      "ds_write_b32 v7, v9 offset:2048\n"
      ".Lgm_Lw1:\n"
      "ds_read_b32 v7, v6 offset:12\n"
      "ds_read_b32 v9, v6 offset:44\n"
      "s_waitcnt lgkmcnt(0)\n"
      "v_cmp_gt_i32 vcc, 1, v7\n"
      "s_cbranch_vccnz .Lgm_Lw1\n"
      "v_readfirstlane_b32 s36, v9\n"
      "s_and_b32 s37, s36, 0xff\n"
      "v_lshrrev_b32 v9, 5, v1\n"
      "v_mul_u32_u24 v14, 128, v9\n"
      "v_and_b32 v9, 31, v0\n"
      "v_lshl_add_u32 v14, v9, 2, v14\n"
      "s_mov_b32 s44, s6\n"
      "s_movk_i32 s7, 0xff\n"
      "v_mov_b32 v9, s37\n"
      "s_waitcnt vmcnt(12)\n"
      "ds_read2_b64 v[16:19], v15 offset0:0 offset1:4\n"
      "ds_read_b64_tr_b16 v[32:33], v10 offset:0\n"
      "ds_read_b64_tr_b16 v[34:35], v10 offset:2112\n"
      "ds_read_b64_tr_b16 v[36:37], v11 offset:0\n"
      "ds_read_b64_tr_b16 v[38:39], v11 offset:2112\n"
      "ds_read_b64_tr_b16 v[40:41], v12 offset:0\n"
      "ds_read_b64_tr_b16 v[42:43], v12 offset:2112\n"
      "ds_read_b64_tr_b16 v[44:45], v13 offset:0\n"
      "ds_read_b64_tr_b16 v[46:47], v13 offset:2112\n"
      "s_waitcnt lgkmcnt(6)\n"
      "v_mfma_f32_16x16x32_bf16 v[48:51], v[16:19], v[32:35], 0\n"
      "s_waitcnt lgkmcnt(4)\n"
      "v_mfma_f32_16x16x32_bf16 v[52:55], v[16:19], v[36:39], 0\n"
      "s_waitcnt lgkmcnt(2)\n"
      "v_mfma_f32_16x16x32_bf16 v[56:59], v[16:19], v[40:43], 0\n"
      "s_waitcnt lgkmcnt(0)\n"
      "v_mfma_f32_16x16x32_bf16 v[60:63], v[16:19], v[44:47], 0\n"
      "s_waitcnt vmcnt(8)\n"
      "ds_read2_b64 v[20:23], v15 offset0:8 offset1:12\n"
      "ds_read_b64_tr_b16 v[32:33], v10 offset:4224\n"
      "ds_read_b64_tr_b16 v[34:35], v10 offset:6336\n"
      "ds_read_b64_tr_b16 v[36:37], v11 offset:4224\n"
      "ds_read_b64_tr_b16 v[38:39], v11 offset:6336\n"
      "ds_read_b64_tr_b16 v[40:41], v12 offset:4224\n"
      "ds_read_b64_tr_b16 v[42:43], v12 offset:6336\n"
      "ds_read_b64_tr_b16 v[44:45], v13 offset:4224\n"
      "ds_read_b64_tr_b16 v[46:47], v13 offset:6336\n"
      "s_waitcnt lgkmcnt(6)\n"
      "v_mfma_f32_16x16x32_bf16 v[48:51], v[20:23], v[32:35], v[48:51]\n"
      "s_waitcnt lgkmcnt(4)\n"
      "v_mfma_f32_16x16x32_bf16 v[52:55], v[20:23], v[36:39], v[52:55]\n"
      "s_waitcnt lgkmcnt(2)\n"
      "v_mfma_f32_16x16x32_bf16 v[56:59], v[20:23], v[40:43], v[56:59]\n"
      "s_waitcnt lgkmcnt(0)\n"
      "v_mfma_f32_16x16x32_bf16 v[60:63], v[20:23], v[44:47], v[60:63]\n"
      "s_waitcnt vmcnt(4)\n"
      "ds_read2_b64 v[24:27], v15 offset0:16 offset1:20\n"
      "ds_read_b64_tr_b16 v[32:33], v10 offset:8448\n"
      "ds_read_b64_tr_b16 v[34:35], v10 offset:10560\n"
      "ds_read_b64_tr_b16 v[36:37], v11 offset:8448\n"
      "ds_read_b64_tr_b16 v[38:39], v11 offset:10560\n"
      "ds_read_b64_tr_b16 v[40:41], v12 offset:8448\n"
      "ds_read_b64_tr_b16 v[42:43], v12 offset:10560\n"
      "ds_read_b64_tr_b16 v[44:45], v13 offset:8448\n"
      "ds_read_b64_tr_b16 v[46:47], v13 offset:10560\n"
      "s_waitcnt lgkmcnt(6)\n"
      "v_mfma_f32_16x16x32_bf16 v[48:51], v[24:27], v[32:35], v[48:51]\n"
      "s_waitcnt lgkmcnt(4)\n"
      "v_mfma_f32_16x16x32_bf16 v[52:55], v[24:27], v[36:39], v[52:55]\n"
      "s_waitcnt lgkmcnt(2)\n"
      "v_mfma_f32_16x16x32_bf16 v[56:59], v[24:27], v[40:43], v[56:59]\n"
      "s_waitcnt lgkmcnt(0)\n"
      "v_mfma_f32_16x16x32_bf16 v[60:63], v[24:27], v[44:47], v[60:63]\n"
      "s_waitcnt vmcnt(0)\n"
      "ds_read2_b64 v[28:31], v15 offset0:24 offset1:28\n"
      "ds_read_b64_tr_b16 v[32:33], v10 offset:12672\n"
      "ds_read_b64_tr_b16 v[34:35], v10 offset:14784\n"
      "ds_read_b64_tr_b16 v[36:37], v11 offset:12672\n"
      "ds_read_b64_tr_b16 v[38:39], v11 offset:14784\n"
      "ds_read_b64_tr_b16 v[40:41], v12 offset:12672\n"
      "ds_read_b64_tr_b16 v[42:43], v12 offset:14784\n"
      "ds_read_b64_tr_b16 v[44:45], v13 offset:12672\n"
      "ds_read_b64_tr_b16 v[46:47], v13 offset:14784\n"
      "s_waitcnt lgkmcnt(6)\n"
      "v_mfma_f32_16x16x32_bf16 v[48:51], v[28:31], v[32:35], v[48:51]\n"
      "s_waitcnt lgkmcnt(4)\n"
      "v_mfma_f32_16x16x32_bf16 v[52:55], v[28:31], v[36:39], v[52:55]\n"
      "s_waitcnt lgkmcnt(2)\n"
      "v_mfma_f32_16x16x32_bf16 v[56:59], v[28:31], v[40:43], v[56:59]\n"
      "s_waitcnt lgkmcnt(0)\n"
      "v_mfma_f32_16x16x32_bf16 v[60:63], v[28:31], v[44:47], v[60:63]\n"
      "s_nop 3\n"
      "v_mov_b32_dpp v48, v52 quad_perm:[0,1,2,3] row_mask:0x2 bank_mask:0xf\n"
      "s_nop 1\n"
      "v_mov_b32_dpp v48, v56 quad_perm:[0,1,2,3] row_mask:0x4 bank_mask:0xf\n"
      "s_nop 1\n"
      "v_mov_b32_dpp v48, v60 quad_perm:[0,1,2,3] row_mask:0x8 bank_mask:0xf\n"
      "v_bfi_b32 v48, s7, v9, v48\n"
      "buffer_store_dword v48, v14, s[32:35], s44 offen sc1\n"
      "s_setprio 0\n"
      "v_and_b32 v12, 31, v0\n"
      "v_lshlrev_b32 v12, 7, v12\n"
      "v_lshrrev_b32 v7, 5, v1\n"
      "v_lshl_add_u32 v12, v7, 3, v12\n"
      "v_lshlrev_b32 v14, 1, v1\n"
      "v_lshlrev_b32 v15, 2, v7\n"
      "s_lshr_b32 s29, s41, 1\n"
      "s_addk_i32 s29, 18\n"
      "s_lshl_b32 s29, s29, 13\n"
      "s_and_b32 s7, s41, 1\n"
      "s_lshl_b32 s7, s7, 12\n"
      "s_add_u32 s29, s29, s7\n"
      "s_and_b32 s7, s5, 1\n"
      "s_lshl4_add_u32 s29, s7, s29\n"
      ".Lgm_Lt2:\n"
      "ds_read_b32 v7, v6 offset:0\n"
      "ds_read_b32 v61, v6 offset:284\n"
      "s_memrealtime s[86:87]\n"
      "s_waitcnt lgkmcnt(0)\n"
      "s_sub_u32 s39, s86, s84\n"
      "v_readfirstlane_b32 s87, v61\n"
      "s_sub_u32 s39, s39, s87\n"
      "s_cmp_ge_i32 s39, 184\n"
      "s_cbranch_scc1 .Lgm_Lu3\n"
      "v_cmp_gt_i32 vcc, 56, v7\n"
      "s_cbranch_vccnz .Lgm_Lt2\n"
      ".Lgm_Lu3:\n"
      "ds_write_b32 v6, v8 offset:264\n"
      ".Lgm_Lw4:\n"
      "ds_read_b32 v7, v6 offset:4\n"
      "s_waitcnt lgkmcnt(0)\n"
      "v_cmp_gt_i32 vcc, 1, v7\n"
      "s_cbranch_vccnz .Lgm_Lw4\n"
      ".Lgm_Lt5:\n"
      "ds_read_b32 v7, v6 offset:0\n"
      "ds_read_b32 v61, v6 offset:292\n"
      "s_memrealtime s[86:87]\n"
      "s_waitcnt lgkmcnt(0)\n"
      "s_sub_u32 s39, s86, s84\n"
      "v_readfirstlane_b32 s87, v61\n"
      "s_sub_u32 s39, s39, s87\n"
      "s_cmp_ge_i32 s39, 372\n"
      "s_cbranch_scc1 .Lgm_Lu6\n"
      "v_cmp_gt_i32 vcc, 88, v7\n"
      "s_cbranch_vccnz .Lgm_Lt5\n"
      ".Lgm_Lu6:\n"
      "s_mov_b32 s39, 2097152\n"
      ".Lgm_Lp7:\n"
      "s_sub_u32 s39, s39, 1\n"
      "s_cbranch_scc1 .Lgm_Ld8\n"
      "buffer_load_dwordx2 v[28:29], v12, s[32:35], s29 offen sc1\n"
      "s_waitcnt vmcnt(0)\n"
      "v_cmp_ne_u32 vcc, s36, v29\n"
      "s_cbranch_vccnz .Lgm_Lp7\n"
      ".Lgm_Ld8:\n"
      "v_max_f32_dpp v30, v28, v28 quad_perm:[1,0,3,2] row_mask:0xf bank_mask:0xf bound_ctrl:1\n"
      "s_nop 1\n"
      "v_max_f32_dpp v30, v30, v30 quad_perm:[2,3,0,1] row_mask:0xf bank_mask:0xf bound_ctrl:1\n"
      "s_nop 1\n"
      "v_max_f32_dpp v30, v30, v30 row_ror:4 row_mask:0xf bank_mask:0xf bound_ctrl:1\n"
      "s_nop 1\n"
      "v_max_f32_dpp v30, v30, v30 row_ror:8 row_mask:0xf bank_mask:0xf bound_ctrl:1\n"
      "v_mov_b32 v31, v30\n"
      "s_nop 1\n"
      "v_permlane16_swap_b32 v30, v31\n"
      "v_max_f32 v30, v30, v31\n"
      "v_sub_f32 v28, v28, v30\n"
      "v_exp_f32 v28, v28\n"
      "s_nop 0\n"
      "v_cvt_pk_bf16_f32 v28, 0, v28\n"
      "ds_write_b16_d16_hi v14, v28 offset:1792\n"
      "ds_write_b32 v6, v8 offset:260\n"
      "v_add_f32_dpp v30, v28, v28 quad_perm:[1,0,3,2] row_mask:0xf bank_mask:0xf bound_ctrl:1\n"
      "s_nop 1\n"
      "v_add_f32_dpp v30, v30, v30 quad_perm:[2,3,0,1] row_mask:0xf bank_mask:0xf bound_ctrl:1\n"
      "s_nop 1\n"
      "v_add_f32_dpp v30, v30, v30 row_ror:4 row_mask:0xf bank_mask:0xf bound_ctrl:1\n"
      "s_nop 1\n"
      "v_add_f32_dpp v30, v30, v30 row_ror:8 row_mask:0xf bank_mask:0xf bound_ctrl:1\n"
      "v_mov_b32 v31, v30\n"
      "s_nop 1\n"
      "v_permlane16_swap_b32 v30, v31\n"
      "v_add_f32 v30, v30, v31\n"
      "v_rcp_f32 v30, v30\n"
      "s_nop 0\n"
      "ds_write_b32 v15, v30 offset:272\n"
      "ds_write_b32 v6, v8 offset:268\n"
      "s_endpgm\n"
      ".p2align 6\n"
      ".Lgm_LTL:\n"
      "s_branch .Lgm_LA\n"
      ".Lgm_LLOAD:\n"
      "s_load_dwordx2 s[12:13], s[0:1], 32\n"
      "s_load_dwordx2 s[16:17], s[0:1], 40\n"
      "s_load_dwordx2 s[24:25], s[0:1], 64\n"
      "s_load_dwordx2 s[28:29], s[0:1], 72\n"
      "s_setprio 3\n"
      "s_getpc_b64 s[80:81]\n"
      ".Lgm_LPC:\n"
      "s_branch .Lgm_LLOAD2\n"
      ".Lgm_LSUB:\n"
      "s_addk_i32 s87, 150\n"
      ".Lgm_LSUBL:\n"
      "ds_read_b32 v9, v6 offset:280\n"
      "s_memrealtime s[84:85]\n"
      "s_getreg_b32 s88, hwreg(HW_REG_IB_STS)\n"
      "s_bfe_u32 s89, s88, 0x20016\n"
      "s_and_b32 s88, s88, 15\n"
      "s_lshl4_add_u32 s88, s89, s88\n"
      "s_sub_u32 s89, s86, s88\n"
      "v_mov_b32 v8, s89\n"
      "ds_write_b32 v6, v8 offset:0\n"
      "s_cmp_le_i32 s88, s90\n"
      "s_cbranch_scc1 .Lgm_LSUBR\n"
      "s_waitcnt lgkmcnt(0)\n"
      "s_cmp_gt_u32 s88, 56\n"
      "s_cbranch_scc1 .Lgm_LSUBL\n"
      "v_readfirstlane_b32 s89, v9\n"
      "s_sub_u32 s89, s84, s89\n"
      "s_mul_i32 s89, s89, 10\n"
      "s_sub_u32 s89, s89, s87\n"
      "s_cmp_lt_i32 s89, 0\n"
      "s_cbranch_scc1 .Lgm_LSUBL\n"
      ".Lgm_LSUBR:\n"
      "s_addk_i32 s86, 4\n"
      "s_setpc_b64 s[82:83]\n"
      ".Lgm_LLOAD2:\n"
      "s_add_u32 s80, s80, 4\n"
      "s_addc_u32 s81, s81, 0\n"
      "v_lshlrev_b32 v7, 2, v3\n"
      "v_xor_b32 v7, v7, v2\n"
      "v_lshlrev_b32 v7, 4, v7\n"
      "v_lshl_or_b32 v14, v3, 8, v7\n"
      "v_lshl_or_b32 v22, v3, 10, v7\n"
      "s_brev_b32 s14, 16\n"
      "s_mov_b64 s[18:19], s[14:15]\n"
      "s_mov_b32 s26, 0x400000\n"
      "s_mov_b32 s27, s35\n"
      "s_cmp_lt_u32 s5, 28\n"
      "s_cselect_b32 s30, 0x1c0000, 0\n"
      "s_mov_b32 s31, s35\n"
      "s_lshl_b32 s45, s4, 24\n"
      "s_add_u32 s45, s45, 15728640\n"
      "s_lshl_b32 s7, s5, 10\n"
      "s_or_b32 s45, s45, s7\n"
      "s_xor_b32 s46, s45, 0x2000\n"
      "s_lshl_b32 s48, s41, 19\n"
      "s_and_b32 s7, s5, 1\n"
      "s_lshl_b32 s7, s7, 18\n"
      "s_or_b32 s48, s48, s7\n"
      "s_bfe_u32 s7, s5, 0x20001\n"
      "s_lshl_b32 s7, s7, 15\n"
      "s_or_b32 s48, s48, s7\n"
      "s_lshl_b32 s7, s43, 8\n"
      "s_or_b32 s48, s48, s7\n"
      "s_lshl1_add_u32 s49, s5, s42\n"
      "s_lshl_b32 s49, s49, 15\n"
      "s_or_b32 s49, s49, s7\n"
      "s_mov_b32 s86, 0\n"
      "v_add_u32 v15, 31712, v14\n"
      "v_add_u32 v23, 3040, v22\n"
      "v_add_u32 v16, 63424, v14\n"
      "v_add_u32 v24, 6080, v22\n"
      "v_add_u32 v17, 95136, v14\n"
      "v_add_u32 v25, 9120, v22\n"
      "s_waitcnt lgkmcnt(0)\n"
      "s_mul_i32 s87, s84, 10\n"
      "s_add_u32 s87, s87, 490\n"
      "s_barrier\n"
      "s_movk_i32 s90, -1\n"
      "s_mov_b32 m0, 33536\n"
      "s_swappc_b64 s[82:83], s[80:81]\n"
      "buffer_load_dwordx4 v14, s[12:15], s45 offen nt lds\n"
      "buffer_load_dwordx4 v15, s[12:15], s45 offen offset:1056 nt lds\n"
      "buffer_load_dwordx4 v16, s[12:15], s45 offen offset:2112 nt lds\n"
      "buffer_load_dwordx4 v17, s[12:15], s45 offen offset:3168 nt lds\n"
      "s_addk_i32 m0, 4224\n"
      "s_add_u32 s7, s45, 131072\n"
      "s_swappc_b64 s[82:83], s[80:81]\n"
      "buffer_load_dwordx4 v14, s[12:15], s7 offen nt lds\n"
      "buffer_load_dwordx4 v15, s[12:15], s7 offen offset:1056 nt lds\n"
      "buffer_load_dwordx4 v16, s[12:15], s7 offen offset:2112 nt lds\n"
      "buffer_load_dwordx4 v17, s[12:15], s7 offen offset:3168 nt lds\n"
      "s_addk_i32 m0, 4224\n"
      "s_add_u32 s7, s45, 262144\n"
      "s_swappc_b64 s[82:83], s[80:81]\n"
      "buffer_load_dwordx4 v14, s[12:15], s7 offen nt lds\n"
      "buffer_load_dwordx4 v15, s[12:15], s7 offen offset:1056 nt lds\n"
      "buffer_load_dwordx4 v16, s[12:15], s7 offen offset:2112 nt lds\n"
      "buffer_load_dwordx4 v17, s[12:15], s7 offen offset:3168 nt lds\n"
      "s_movk_i32 s90, 8\n"
      "s_addk_i32 m0, 4224\n"
      "s_add_u32 s7, s45, 393216\n"
      "s_swappc_b64 s[82:83], s[80:81]\n"
      "buffer_load_dwordx4 v14, s[12:15], s7 offen nt lds\n"
      "buffer_load_dwordx4 v15, s[12:15], s7 offen offset:1056 nt lds\n"
      "buffer_load_dwordx4 v16, s[12:15], s7 offen offset:2112 nt lds\n"
      "buffer_load_dwordx4 v17, s[12:15], s7 offen offset:3168 nt lds\n"
      "s_addk_i32 m0, 4224\n"
      "s_add_u32 s7, s46, 524288\n"
      "s_swappc_b64 s[82:83], s[80:81]\n"
      "buffer_load_dwordx4 v14, s[12:15], s7 offen nt lds\n"
      "buffer_load_dwordx4 v15, s[12:15], s7 offen offset:1056 nt lds\n"
      "buffer_load_dwordx4 v16, s[12:15], s7 offen offset:2112 nt lds\n"
      "buffer_load_dwordx4 v17, s[12:15], s7 offen offset:3168 nt lds\n"
      "s_addk_i32 m0, 4224\n"
      "s_add_u32 s7, s46, 655360\n"
      "s_swappc_b64 s[82:83], s[80:81]\n"
      "buffer_load_dwordx4 v14, s[12:15], s7 offen nt lds\n"
      "buffer_load_dwordx4 v15, s[12:15], s7 offen offset:1056 nt lds\n"
      "buffer_load_dwordx4 v16, s[12:15], s7 offen offset:2112 nt lds\n"
      "buffer_load_dwordx4 v17, s[12:15], s7 offen offset:3168 nt lds\n"
      "s_addk_i32 m0, 4224\n"
      "s_add_u32 s7, s46, 786432\n"
      "s_swappc_b64 s[82:83], s[80:81]\n"
      "buffer_load_dwordx4 v14, s[12:15], s7 offen nt lds\n"
      "buffer_load_dwordx4 v15, s[12:15], s7 offen offset:1056 nt lds\n"
      "buffer_load_dwordx4 v16, s[12:15], s7 offen offset:2112 nt lds\n"
      "buffer_load_dwordx4 v17, s[12:15], s7 offen offset:3168 nt lds\n"
      "s_addk_i32 m0, 4224\n"
      "s_add_u32 s7, s46, 917504\n"
      "s_swappc_b64 s[82:83], s[80:81]\n"
      "buffer_load_dwordx4 v14, s[12:15], s7 offen nt lds\n"
      "buffer_load_dwordx4 v15, s[12:15], s7 offen offset:1056 nt lds\n"
      "buffer_load_dwordx4 v16, s[12:15], s7 offen offset:2112 nt lds\n"
      "buffer_load_dwordx4 v17, s[12:15], s7 offen offset:3168 nt lds\n"
      "s_addk_i32 s87, 110\n"
      "s_movk_i32 s90, -1\n"
      "s_addk_i32 m0, 4224\n"
      "s_swappc_b64 s[82:83], s[80:81]\n"
      "buffer_load_dwordx4 v14, s[16:19], s45 offen nt lds\n"
      "buffer_load_dwordx4 v15, s[16:19], s45 offen offset:1056 nt lds\n"
      "buffer_load_dwordx4 v16, s[16:19], s45 offen offset:2112 nt lds\n"
      "buffer_load_dwordx4 v17, s[16:19], s45 offen offset:3168 nt lds\n"
      "s_movk_i32 s90, 8\n"
      "s_addk_i32 m0, 4224\n"
      "s_add_u32 s7, s45, 131072\n"
      "s_swappc_b64 s[82:83], s[80:81]\n"
      "buffer_load_dwordx4 v14, s[16:19], s7 offen nt lds\n"
      "buffer_load_dwordx4 v15, s[16:19], s7 offen offset:1056 nt lds\n"
      "buffer_load_dwordx4 v16, s[16:19], s7 offen offset:2112 nt lds\n"
      "buffer_load_dwordx4 v17, s[16:19], s7 offen offset:3168 nt lds\n"
      "s_addk_i32 m0, 4224\n"
      "s_add_u32 s7, s45, 262144\n"
      "s_swappc_b64 s[82:83], s[80:81]\n"
      "buffer_load_dwordx4 v14, s[16:19], s7 offen nt lds\n"
      "buffer_load_dwordx4 v15, s[16:19], s7 offen offset:1056 nt lds\n"
      "buffer_load_dwordx4 v16, s[16:19], s7 offen offset:2112 nt lds\n"
      "buffer_load_dwordx4 v17, s[16:19], s7 offen offset:3168 nt lds\n"
      "s_addk_i32 m0, 4224\n"
      "s_add_u32 s7, s45, 393216\n"
      "s_swappc_b64 s[82:83], s[80:81]\n"
      "buffer_load_dwordx4 v14, s[16:19], s7 offen nt lds\n"
      "buffer_load_dwordx4 v15, s[16:19], s7 offen offset:1056 nt lds\n"
      "buffer_load_dwordx4 v16, s[16:19], s7 offen offset:2112 nt lds\n"
      "buffer_load_dwordx4 v17, s[16:19], s7 offen offset:3168 nt lds\n"
      "s_addk_i32 m0, 4224\n"
      "s_add_u32 s7, s46, 524288\n"
      "s_swappc_b64 s[82:83], s[80:81]\n"
      "buffer_load_dwordx4 v14, s[16:19], s7 offen nt lds\n"
      "buffer_load_dwordx4 v15, s[16:19], s7 offen offset:1056 nt lds\n"
      "buffer_load_dwordx4 v16, s[16:19], s7 offen offset:2112 nt lds\n"
      "buffer_load_dwordx4 v17, s[16:19], s7 offen offset:3168 nt lds\n"
      "s_addk_i32 m0, 4224\n"
      "s_add_u32 s7, s46, 655360\n"
      "s_swappc_b64 s[82:83], s[80:81]\n"
      "buffer_load_dwordx4 v14, s[16:19], s7 offen nt lds\n"
      "buffer_load_dwordx4 v15, s[16:19], s7 offen offset:1056 nt lds\n"
      "buffer_load_dwordx4 v16, s[16:19], s7 offen offset:2112 nt lds\n"
      "buffer_load_dwordx4 v17, s[16:19], s7 offen offset:3168 nt lds\n"
      "s_addk_i32 m0, 4224\n"
      "s_add_u32 s7, s46, 786432\n"
      "s_swappc_b64 s[82:83], s[80:81]\n"
      "buffer_load_dwordx4 v14, s[16:19], s7 offen nt lds\n"
      "buffer_load_dwordx4 v15, s[16:19], s7 offen offset:1056 nt lds\n"
      "buffer_load_dwordx4 v16, s[16:19], s7 offen offset:2112 nt lds\n"
      "buffer_load_dwordx4 v17, s[16:19], s7 offen offset:3168 nt lds\n"
      "s_addk_i32 m0, 4224\n"
      "s_add_u32 s7, s46, 917504\n"
      "s_swappc_b64 s[82:83], s[80:81]\n"
      "buffer_load_dwordx4 v14, s[16:19], s7 offen nt lds\n"
      "buffer_load_dwordx4 v15, s[16:19], s7 offen offset:1056 nt lds\n"
      "buffer_load_dwordx4 v16, s[16:19], s7 offen offset:2112 nt lds\n"
      "buffer_load_dwordx4 v17, s[16:19], s7 offen offset:3168 nt lds\n"
      "s_addk_i32 s87, 165\n"
      "s_movk_i32 s90, 0\n"
      "s_addk_i32 m0, 4224\n"
      "s_swappc_b64 s[82:83], s[80:81]\n"
      "buffer_load_dwordx4 v22, s[28:31], s49 offen nt lds\n"
      "buffer_load_dwordx4 v23, s[28:31], s49 offen offset:1056 nt lds\n"
      "buffer_load_dwordx4 v24, s[28:31], s49 offen offset:2112 nt lds\n"
      "buffer_load_dwordx4 v25, s[28:31], s49 offen offset:3168 nt lds\n"
      "s_addk_i32 s87, -42\n"
      "s_addk_i32 m0, 4224\n"
      "s_add_u32 s7, s49, 16384\n"
      "s_swappc_b64 s[82:83], s[80:81]\n"
      "buffer_load_dwordx4 v22, s[28:31], s7 offen nt lds\n"
      "buffer_load_dwordx4 v23, s[28:31], s7 offen offset:1056 nt lds\n"
      "buffer_load_dwordx4 v24, s[28:31], s7 offen offset:2112 nt lds\n"
      "buffer_load_dwordx4 v25, s[28:31], s7 offen offset:3168 nt lds\n"
      "s_addk_i32 s87, -42\n"
      "s_addk_i32 m0, 4224\n"
      "s_swappc_b64 s[82:83], s[80:81]\n"
      "buffer_load_dwordx4 v22, s[24:27], s48 offen nt lds\n"
      "buffer_load_dwordx4 v23, s[24:27], s48 offen offset:1056 nt lds\n"
      "buffer_load_dwordx4 v24, s[24:27], s48 offen offset:2112 nt lds\n"
      "buffer_load_dwordx4 v25, s[24:27], s48 offen offset:3168 nt lds\n"
      "s_addk_i32 s87, -42\n"
      "s_addk_i32 m0, 4224\n"
      "s_add_u32 s7, s48, 16384\n"
      "s_swappc_b64 s[82:83], s[80:81]\n"
      "buffer_load_dwordx4 v22, s[24:27], s7 offen nt lds\n"
      "buffer_load_dwordx4 v23, s[24:27], s7 offen offset:1056 nt lds\n"
      "buffer_load_dwordx4 v24, s[24:27], s7 offen offset:2112 nt lds\n"
      "buffer_load_dwordx4 v25, s[24:27], s7 offen offset:3168 nt lds\n"
      "s_addk_i32 s87, -42\n"
      "s_addk_i32 m0, 4224\n"
      "s_add_u32 s7, s48, 131072\n"
      "s_swappc_b64 s[82:83], s[80:81]\n"
      "buffer_load_dwordx4 v22, s[24:27], s7 offen nt lds\n"
      "buffer_load_dwordx4 v23, s[24:27], s7 offen offset:1056 nt lds\n"
      "buffer_load_dwordx4 v24, s[24:27], s7 offen offset:2112 nt lds\n"
      "buffer_load_dwordx4 v25, s[24:27], s7 offen offset:3168 nt lds\n"
      "s_addk_i32 s87, -42\n"
      "s_addk_i32 m0, 4224\n"
      "s_add_u32 s7, s48, 147456\n"
      "s_swappc_b64 s[82:83], s[80:81]\n"
      "buffer_load_dwordx4 v22, s[24:27], s7 offen nt lds\n"
      "buffer_load_dwordx4 v23, s[24:27], s7 offen offset:1056 nt lds\n"
      "buffer_load_dwordx4 v24, s[24:27], s7 offen offset:2112 nt lds\n"
      "buffer_load_dwordx4 v25, s[24:27], s7 offen offset:3168 nt lds\n"
      "s_mov_b64 exec, 1\n"
      "s_waitcnt vmcnt(56)\n"
      "v_mov_b32 v8, 32\n"
      "ds_max_i32 v6, v8 offset:0\n"
      "s_waitcnt vmcnt(36)\n"
      "v_mov_b32 v8, 52\n"
      "ds_max_i32 v6, v8 offset:0\n"
      "s_waitcnt vmcnt(32)\n"
      "v_mov_b32 v8, 56\n"
      "ds_max_i32 v6, v8 offset:0\n"
      "s_waitcnt vmcnt(24)\n"
      "v_mov_b32 v8, 64\n"
      "ds_max_i32 v6, v8 offset:0\n"
      "s_waitcnt vmcnt(16)\n"
      "v_mov_b32 v8, 72\n"
      "ds_max_i32 v6, v8 offset:0\n"
      "s_waitcnt vmcnt(4)\n"
      "v_mov_b32 v8, 84\n"
      "ds_max_i32 v6, v8 offset:0\n"
      "s_waitcnt vmcnt(0)\n"
      "v_mov_b32 v8, 88\n"
      "ds_max_i32 v6, v8 offset:0\n"
      "s_endpgm\n"
      ".p2align 12\n"
      ".Lgm_LA:\n"
      "s_barrier\n"
      "s_waitcnt lgkmcnt(0)\n"
      "s_movk_i32 s26, 1024\n"
      "s_mov_b32 s27, s35\n"
      "s_lshr_b32 s6, s3, 6\n"
      "s_and_b32 s6, s6, 3\n"
      "s_lshl_b32 s7, s6, 3\n"
      "v_add_u32 v9, s7, v4\n"
      "v_mul_u32_u24 v9, 1056, v9\n"
      "v_lshl_add_u32 v9, v5, 8, v9\n"
      "v_lshl_add_u32 v9, v3, 4, v9\n"
      "v_add_u32 v9, 33536, v9\n"
      "v_xor_b32 v7, 0, v5\n"
      "v_lshl_add_u32 v10, v7, 6, v9\n"
      "v_xor_b32 v7, 1, v5\n"
      "v_lshl_add_u32 v11, v7, 6, v9\n"
      "v_xor_b32 v7, 2, v5\n"
      "v_lshl_add_u32 v12, v7, 6, v9\n"
      "v_xor_b32 v7, 3, v5\n"
      "v_lshl_add_u32 v13, v7, 6, v9\n"
      "v_mul_u32_u24 v9, 1056, v3\n"
      "v_lshl_add_u32 v9, v4, 8, v9\n"
      "v_lshl_add_u32 v9, v5, 3, v9\n"
      "v_xor_b32 v7, s6, v4\n"
      "v_lshl_add_u32 v9, v7, 6, v9\n"
      "v_add_u32 v14, 67328, v9\n"
      "v_add_u32 v15, 109568, v9\n"
      "v_add_u32 v63, 101120, v9\n"
      "v_and_b32 v7, 31, v0\n"
      "v_lshrrev_b32 v9, 5, v1\n"
      "v_lshlrev_b32 v16, 4, v7\n"
      "v_lshl_add_u32 v16, v9, 11, v16\n"
      "v_lshlrev_b32 v17, 3, v7\n"
      "v_lshl_add_u32 v17, v9, 2, v17\n"
      "s_mul_i32 s7, s6, 320\n"
      "v_add_u32 v17, s7, v17\n"
      "v_lshlrev_b32 v24, 2, v7\n"
      "v_lshl_add_u32 v25, v9, 13, v24\n"
      "v_lshl_add_u32 v24, v9, 12, v24\n"
      "v_mul_u32_u24 v18, 320, v5\n"
      "v_lshl_add_u32 v18, v3, 4, v18\n"
      "v_and_b32 v7, 2, v4\n"
      "v_xor_b32 v7, v7, v5\n"
      "v_lshlrev_b32 v19, 6, v7\n"
      "v_lshl_add_u32 v19, v3, 4, v19\n"
      "v_lshlrev_b32 v20, 5, v5\n"
      "s_lshl_b32 s7, s6, 3\n"
      "v_add_u32 v21, s7, v20\n"
      "v_lshlrev_b32 v22, 6, v5\n"
      "v_lshl_add_u32 v22, v3, 4, v22\n"
      "v_lshl_add_u32 v22, v4, 2, v22\n"
      "s_lshl_b32 s7, s6, 8\n"
      "v_add_u32 v22, s7, v22\n"
      "s_lshl_b32 s7, s6, 2\n"
      "v_mov_b32 v23, s7\n"
      "s_lshl2_add_u32 s48, s4, s6\n"
      "s_addk_i32 s48, 13\n"
      "s_lshl_b32 s48, s48, 14\n"
      "s_lshl_b32 s7, s5, 7\n"
      "s_add_u32 s48, s48, s7\n"
      "s_and_b32 s47, s6, 1\n"
      "s_lshl_b32 s47, s47, 13\n"
      "s_add_u32 s48, s48, s47\n"
      "s_lshl2_add_u32 s49, s43, s6\n"
      "s_addk_i32 s49, 56\n"
      "s_lshl_b32 s49, s49, 14\n"
      "s_add_u32 s49, s49, s7\n"
      "s_lshl_b32 s28, s42, 12\n"
      "s_add_u32 s49, s49, s28\n"
      "v_lshlrev_b32 v26, 3, v2\n"
      "s_lshr_b32 s29, s4, 1\n"
      "s_addk_i32 s29, 18\n"
      "s_lshl_b32 s29, s29, 13\n"
      "s_add_u32 s29, s29, s28\n"
      "s_add_u32 s29, s29, s7\n"
      "v_lshlrev_b32 v27, 3, v3\n"
      "s_add_u32 s10, s4, 2\n"
      "s_lshl_b32 s10, s10, 13\n"
      "s_lshl_b32 s11, s6, 9\n"
      "s_add_u32 s10, s10, s11\n"
      "s_add_u32 s11, s10, 4096\n"
      "s_lshl_b32 s12, s6, 3\n"
      "s_lshr_b32 s12, 0x20181008, s12\n"
      "s_and_b32 s12, s12, 0xff\n"
      ".Lgm_Lw9:\n"
      "ds_read_b32 v7, v6 offset:12\n"
      "ds_read_b32 v9, v6 offset:44\n"
      "s_waitcnt lgkmcnt(0)\n"
      "v_cmp_gt_i32 vcc, 1, v7\n"
      "s_cbranch_vccnz .Lgm_Lw9\n"
      "v_readfirstlane_b32 s36, v9\n"
      "s_and_b32 s37, s36, 0xff\n"
      ".Lgm_Lw10:\n"
      "ds_read_b32 v7, v6 offset:264\n"
      "s_waitcnt lgkmcnt(0)\n"
      "v_cmp_gt_i32 vcc, 1, v7\n"
      "s_cbranch_vccnz .Lgm_Lw10\n"
      "buffer_load_dwordx4 v[28:31], v16, s[32:35], s10 offen sc1\n"
      "buffer_load_dwordx4 v[64:67], v16, s[32:35], s11 offen sc1\n"
      "s_sleep 10\n"
      "ds_read_b32 v62, v6 offset:0\n"
      "ds_read_b128 v[68:71], v10 offset:0\n"
      "ds_read_b128 v[84:87], v10 offset:4224\n"
      "ds_read_b128 v[72:75], v11 offset:0\n"
      "ds_read_b128 v[88:91], v11 offset:4224\n"
      "ds_read_b128 v[76:79], v12 offset:0\n"
      "ds_read_b128 v[92:95], v12 offset:4224\n"
      "ds_read_b128 v[80:83], v13 offset:0\n"
      "ds_read_b128 v[96:99], v13 offset:4224\n"
      "s_mov_b32 s39, 2097152\n"
      "s_branch .Lgm_Lc11\n"
      ".Lgm_Lp12:\n"
      "s_sub_u32 s39, s39, 1\n"
      "s_cbranch_scc1 .Lgm_Ld13\n"
      "buffer_load_dwordx4 v[28:31], v16, s[32:35], s10 offen sc1\n"
      "buffer_load_dwordx4 v[64:67], v16, s[32:35], s11 offen sc1\n"
      ".Lgm_Lc11:\n"
      "s_waitcnt vmcnt(0)\n"
      "v_xor_b32 v28, s37, v28\n"
      "v_xor_b32 v29, s37, v29\n"
      "v_xor_b32 v30, s37, v30\n"
      "v_xor_b32 v31, s37, v31\n"
      "v_xor_b32 v64, s37, v64\n"
      "v_xor_b32 v65, s37, v65\n"
      "v_xor_b32 v66, s37, v66\n"
      "v_xor_b32 v67, s37, v67\n"
      "v_or3_b32 v7, v28, v29, v30\n"
      "v_or3_b32 v7, v7, v31, v64\n"
      "v_or3_b32 v7, v7, v65, v66\n"
      "v_or_b32 v7, v7, v67\n"
      "v_and_b32 v7, 0xff, v7\n"
      "v_cmp_ne_u32 vcc, 0, v7\n"
      "v_pk_add_f32 v[28:29], v[28:29], v[64:65]\n"
      "v_pk_add_f32 v[30:31], v[30:31], v[66:67]\n"
      "s_cbranch_vccnz .Lgm_Lp12\n"
      ".Lgm_Ld13:\n"
      "s_nop 0\n"
      "v_permlane32_swap_b32 v28, v30\n"
      "v_permlane32_swap_b32 v29, v31\n"
      "v_pk_add_f32 v[28:29], v[28:29], v[30:31]\n"
      "v_cvt_pk_bf16_f32 v28, v28, v29\n"
      "ds_write_b32 v17, v28 offset:512\n"
      "ds_write_b32 v23, v8 offset:16\n"
      ".Lgm_Lw14:\n"
      "ds_read_b128 v[28:31], v6 offset:16\n"
      "ds_read_b128 v[32:35], v18 offset:512\n"
      "ds_read_b128 v[36:39], v18 offset:576\n"
      "ds_read_b128 v[40:43], v18 offset:640\n"
      "ds_read_b128 v[44:47], v18 offset:704\n"
      "s_waitcnt lgkmcnt(4)\n"
      "v_and_b32 v28, v28, v29\n"
      "v_bitop3_b32 v28, v28, v30, v31 bitop3:0x80\n"
      "v_cmp_eq_u32 vcc, 0, v28\n"
      "s_cbranch_vccnz .Lgm_Lw14\n"
      "v_cmp_le_i32 vcc, s12, v62\n"
      "s_cbranch_vccnz .Lgm_LKOK\n"
      ".Lgm_Lw15:\n"
      "ds_read_b32 v7, v6 offset:0\n"
      "ds_read_b128 v[68:71], v10 offset:0\n"
      "ds_read_b128 v[84:87], v10 offset:4224\n"
      "ds_read_b128 v[72:75], v11 offset:0\n"
      "ds_read_b128 v[88:91], v11 offset:4224\n"
      "ds_read_b128 v[76:79], v12 offset:0\n"
      "ds_read_b128 v[92:95], v12 offset:4224\n"
      "ds_read_b128 v[80:83], v13 offset:0\n"
      "ds_read_b128 v[96:99], v13 offset:4224\n"
      "s_waitcnt lgkmcnt(0)\n"
      "v_cmp_gt_i32 vcc, s12, v7\n"
      "s_cbranch_vccnz .Lgm_Lw15\n"
      ".Lgm_LKOK:\n"
      "s_waitcnt lgkmcnt(0)\n"
      "v_mfma_f32_16x16x32_bf16 v[48:51], v[68:71], v[32:35], 0\n"
      "v_mfma_f32_16x16x32_bf16 v[52:55], v[84:87], v[32:35], 0\n"
      "v_mfma_f32_16x16x32_bf16 v[48:51], v[72:75], v[36:39], v[48:51]\n"
      "v_mfma_f32_16x16x32_bf16 v[52:55], v[88:91], v[36:39], v[52:55]\n"
      "v_mfma_f32_16x16x32_bf16 v[48:51], v[76:79], v[40:43], v[48:51]\n"
      "v_mfma_f32_16x16x32_bf16 v[52:55], v[92:95], v[40:43], v[52:55]\n"
      "v_mfma_f32_16x16x32_bf16 v[48:51], v[80:83], v[44:47], v[48:51]\n"
      "v_mfma_f32_16x16x32_bf16 v[52:55], v[96:99], v[44:47], v[52:55]\n"
      "s_mov_b32 s14, 0x3db504f3\n"
      "s_mov_b32 s15, s14\n"
      "s_mov_b32 s20, 0x3fb8aa3b\n"
      "s_mov_b32 s21, s20\n"
      "s_nop 2\n"
      "v_mov_b32_dpp v48, v50 quad_perm:[0,1,2,3] row_mask:0xf bank_mask:0x2\n"
      "v_mov_b32_dpp v49, v51 quad_perm:[0,1,2,3] row_mask:0xf bank_mask:0x2\n"
      "s_nop 0\n"
      "v_mov_b32_dpp v48, v52 quad_perm:[0,1,2,3] row_mask:0xf bank_mask:0x4\n"
      "v_mov_b32_dpp v49, v53 quad_perm:[0,1,2,3] row_mask:0xf bank_mask:0x4\n"
      "s_nop 0\n"
      "v_mov_b32_dpp v48, v54 quad_perm:[0,1,2,3] row_mask:0xf bank_mask:0x8\n"
      "v_mov_b32_dpp v49, v55 quad_perm:[0,1,2,3] row_mask:0xf bank_mask:0x8\n"
      "v_cvt_pk_bf16_f32 v48, 0, v48\n"
      "v_cvt_pk_bf16_f32 v49, 0, v49\n"
      "v_pk_mul_f32 v[48:49], v[48:49], s[14:15]\n"
      "v_cvt_pk_bf16_f32 v48, 0, v48\n"
      "v_cvt_pk_bf16_f32 v49, 0, v49\n"
      "v_max_f32 v28, v48, v49\n"
      "s_nop 1\n"
      "v_max_f32_dpp v28, v28, v28 row_ror:4 row_mask:0xf bank_mask:0xf bound_ctrl:1\n"
      "s_nop 1\n"
      "v_max_f32_dpp v28, v28, v28 row_ror:8 row_mask:0xf bank_mask:0xf bound_ctrl:1\n"
      "v_mov_b32 v29, v28\n"
      "s_nop 1\n"
      "v_permlane16_swap_b32 v28, v29\n"
      "v_max_f32 v28, v28, v29\n"
      "v_mov_b32 v29, v28\n"
      "s_nop 1\n"
      "v_permlane32_swap_b32 v28, v29\n"
      "v_max_f32 v28, v28, v29\n"
      "v_pk_add_f32 v[48:49], v[48:49], v[28:29] op_sel_hi:[1,0] neg_lo:[0,1] neg_hi:[0,1]\n"
      "v_pk_mul_f32 v[48:49], v[48:49], s[20:21]\n"
      "v_mul_f32 v28, s20, v28\n"
      "v_exp_f32 v48, v48\n"
      "v_exp_f32 v49, v49\n"
      "s_nop 0\n"
      "v_add_f32 v29, v48, v49\n"
      "s_nop 1\n"
      "v_add_f32_dpp v29, v29, v29 row_ror:4 row_mask:0xf bank_mask:0xf bound_ctrl:1\n"
      "s_nop 1\n"
      "v_add_f32_dpp v29, v29, v29 row_ror:8 row_mask:0xf bank_mask:0xf bound_ctrl:1\n"
      "v_mov_b32 v30, v29\n"
      "s_nop 1\n"
      "v_permlane16_swap_b32 v29, v30\n"
      "v_add_f32 v29, v29, v30\n"
      "v_mov_b32 v30, v29\n"
      "s_nop 1\n"
      "v_permlane32_swap_b32 v29, v30\n"
      "v_add_f32 v29, v29, v30\n"
      "s_mov_b64 exec, 15\n"
      "ds_write_b64 v21, v[28:29] offset:384\n"
      "s_mov_b64 exec, -1\n"
      "v_mov_b32 v43, s36\n"
      ".Lgm_Lw16:\n"
      "ds_read_b128 v[32:35], v20 offset:384\n"
      "ds_read_b128 v[36:39], v20 offset:400\n"
      "s_waitcnt lgkmcnt(0)\n"
      "v_min3_u32 v30, v33, v35, v37\n"
      "v_min_u32 v30, v30, v39\n"
      "v_cmp_eq_u32 vcc, 0, v30\n"
      "v_max3_f32 v40, v32, v34, v36\n"
      "s_cbranch_vccnz .Lgm_Lw16\n"
      "v_max_f32 v40, v40, v38\n"
      "v_sub_f32 v32, v32, v40\n"
      "v_sub_f32 v34, v34, v40\n"
      "v_sub_f32 v36, v36, v40\n"
      "v_sub_f32 v38, v38, v40\n"
      "v_exp_f32 v32, v32\n"
      "v_exp_f32 v34, v34\n"
      "v_exp_f32 v36, v36\n"
      "v_exp_f32 v38, v38\n"
      "v_mul_f32 v29, v32, v33\n"
      "v_fmac_f32 v29, v34, v35\n"
      "v_fmac_f32 v29, v36, v37\n"
      "v_fmac_f32 v29, v38, v39\n"
      "s_cmp_eq_u32 s6, 0\n"
      "s_cbranch_scc0 .Lgm_LNOLSE\n"
      "v_log_f32 v42, v29\n"
      "s_mov_b64 exec, 0xffff\n"
      "v_add_f32 v42, v42, v40\n"
      "buffer_store_dwordx2 v[42:43], v26, s[32:35], s29 offen sc1\n"
      "s_mov_b64 exec, -1\n"
      "ds_write_b32 v6, v8 offset:4\n"
      ".Lgm_LNOLSE:\n"
      "v_sub_f32 v28, v28, v40\n"
      "v_rcp_f32 v30, v29\n"
      "v_exp_f32 v28, v28\n"
      "s_nop 0\n"
      "v_mul_f32 v28, v28, v30\n"
      "v_pk_mul_f32 v[48:49], v[48:49], v[28:29] op_sel_hi:[1,0]\n"
      "v_cvt_pk_bf16_f32 v32, v48, v49\n"
      "ds_write_b32 v22, v32 offset:4096\n"
      "ds_write_b32 v23, v8 offset:48\n"
      ".Lgm_Lw17:\n"
      "ds_read_b128 v[28:31], v6 offset:48\n"
      "ds_read_b32 v7, v6 offset:288\n"
      "ds_read_b128 v[32:35], v19 offset:4096\n"
      "ds_read_b128 v[36:39], v19 offset:4352\n"
      "ds_read_b128 v[40:43], v19 offset:4608\n"
      "ds_read_b128 v[44:47], v19 offset:4864\n"
      "s_waitcnt lgkmcnt(4)\n"
      "v_bitop3_b32 v28, v28, v29, v7 bitop3:0x80\n"
      "v_bitop3_b32 v28, v28, v30, v31 bitop3:0x80\n"
      "v_cmp_eq_u32 vcc, 0, v28\n"
      "s_cbranch_vccnz .Lgm_Lw17\n"
      "s_waitcnt lgkmcnt(0)\n"
      ".Lgm_Lw18:\n"
      "ds_read_b32 v7, v6 offset:0\n"
      "ds_read_b64_tr_b16 v[64:65], v14 offset:0\n"
      "ds_read_b64_tr_b16 v[66:67], v14 offset:4224\n"
      "ds_read_b64_tr_b16 v[68:69], v14 offset:32\n"
      "ds_read_b64_tr_b16 v[70:71], v14 offset:4256\n"
      "ds_read_b64_tr_b16 v[72:73], v14 offset:8448\n"
      "ds_read_b64_tr_b16 v[74:75], v14 offset:12672\n"
      "ds_read_b64_tr_b16 v[76:77], v14 offset:8480\n"
      "ds_read_b64_tr_b16 v[78:79], v14 offset:12704\n"
      "ds_read_b64_tr_b16 v[80:81], v14 offset:16896\n"
      "ds_read_b64_tr_b16 v[82:83], v14 offset:21120\n"
      "ds_read_b64_tr_b16 v[84:85], v14 offset:16928\n"
      "ds_read_b64_tr_b16 v[86:87], v14 offset:21152\n"
      "s_waitcnt lgkmcnt(0)\n"
      "v_cmp_gt_i32 vcc, 56, v7\n"
      "s_cbranch_vccnz .Lgm_Lw18\n"
      "v_mfma_f32_16x16x32_bf16 v[48:51], v[32:35], v[64:67], 0\n"
      "v_mfma_f32_16x16x32_bf16 v[52:55], v[32:35], v[68:71], 0\n"
      "v_mfma_f32_16x16x32_bf16 v[48:51], v[36:39], v[72:75], v[48:51]\n"
      "v_mfma_f32_16x16x32_bf16 v[52:55], v[36:39], v[76:79], v[52:55]\n"
      "v_mfma_f32_16x16x32_bf16 v[48:51], v[40:43], v[80:83], v[48:51]\n"
      "v_mfma_f32_16x16x32_bf16 v[52:55], v[40:43], v[84:87], v[52:55]\n"
      ".Lgm_Lw19:\n"
      "ds_read_b32 v7, v6 offset:0\n"
      "ds_read_b64_tr_b16 v[88:89], v14 offset:25344\n"
      "ds_read_b64_tr_b16 v[90:91], v14 offset:29568\n"
      "ds_read_b64_tr_b16 v[92:93], v14 offset:25376\n"
      "ds_read_b64_tr_b16 v[94:95], v14 offset:29600\n"
      "s_waitcnt lgkmcnt(0)\n"
      "v_cmp_gt_i32 vcc, 64, v7\n"
      "s_cbranch_vccnz .Lgm_Lw19\n"
      "v_mfma_f32_16x16x32_bf16 v[48:51], v[44:47], v[88:91], v[48:51]\n"
      "v_mfma_f32_16x16x32_bf16 v[52:55], v[44:47], v[92:95], v[52:55]\n"
      "s_nop 6\n"
      "v_cvt_pk_bf16_f32 v56, v48, v49\n"
      "v_cvt_pk_bf16_f32 v57, v52, v53\n"
      "s_nop 1\n"
      "v_mov_b32_dpp v56, v57 quad_perm:[0,1,2,3] row_mask:0xa bank_mask:0xf\n"
      "v_min_u32 v56, -2, v56\n"
      "buffer_store_dword v56, v24, s[32:35], s48 offen sc1\n"
      "ds_write_b32 v6, v8 offset:8\n"
      "s_sub_u32 s14, s5, 24\n"
      "s_lshl3_add_u32 s14, s42, s14\n"
      "s_xor_b32 s15, s43, 1\n"
      "s_lshr_b32 s16, s14, 2\n"
      "s_lshl2_add_u32 s16, s15, s16\n"
      "s_addk_i32 s16, 56\n"
      "s_lshl_b32 s16, s16, 14\n"
      "s_and_b32 s17, s14, 3\n"
      "s_lshl_b32 s17, s17, 5\n"
      "s_add_u32 s16, s16, s17\n"
      "s_lshl4_add_u32 s18, s15, s14\n"
      "s_lshl_b32 s18, s18, 4\n"
      "v_lshrrev_b32 v16, 3, v1\n"
      "v_lshlrev_b32 v16, 7, v16\n"
      "v_and_b32 v7, 7, v0\n"
      "v_lshl_add_u32 v16, v7, 2, v16\n"
      "v_lshlrev_b32 v17, 1, v1\n"
      "s_movk_i32 s7, 0xff\n"
      "v_mov_b32 v9, s37\n"
      ".Lgm_Lw20:\n"
      "ds_read_b32 v7, v6 offset:256\n"
      "s_waitcnt lgkmcnt(0)\n"
      "v_cmp_gt_i32 vcc, 1, v7\n"
      "s_cbranch_vccnz .Lgm_Lw20\n"
      "v_and_b32 v7, 0x2000, v7\n"
      "v_add_u32 v16, v16, v7\n"
      "s_add_u32 s17, s16, 4096\n"
      "s_cmp_lt_u32 s5, 24\n"
      "s_cbranch_scc1 .Lgm_LNOPRE\n"
      "s_cmp_lg_u32 s6, 0\n"
      "s_cbranch_scc1 .Lgm_LNOPRE\n"
      "buffer_load_dword v40, v16, s[32:35], s16 offen offset:0 sc1\n"
      "buffer_load_dword v41, v16, s[32:35], s16 offen offset:1024 sc1\n"
      "buffer_load_dword v42, v16, s[32:35], s16 offen offset:2048 sc1\n"
      "buffer_load_dword v43, v16, s[32:35], s16 offen offset:3072 sc1\n"
      "buffer_load_dword v44, v16, s[32:35], s17 offen offset:0 sc1\n"
      "buffer_load_dword v45, v16, s[32:35], s17 offen offset:1024 sc1\n"
      "buffer_load_dword v46, v16, s[32:35], s17 offen offset:2048 sc1\n"
      "buffer_load_dword v47, v16, s[32:35], s17 offen offset:3072 sc1\n"
      "s_mov_b64 exec, 0xff\n"
      "buffer_store_short v6, v17, s[24:27], s18 offen\n"
      "s_mov_b64 exec, -1\n"
      ".Lgm_LNOPRE:\n"
      ".Lgm_Lw21:\n"
      "ds_read_b32 v7, v6 offset:0\n"
      "ds_read_b64_tr_b16 v[80:81], v63 offset:0\n"
      "ds_read_b64_tr_b16 v[82:83], v63 offset:4224\n"
      "ds_read_b64_tr_b16 v[84:85], v63 offset:32\n"
      "ds_read_b64_tr_b16 v[86:87], v63 offset:4256\n"
      "s_waitcnt lgkmcnt(0)\n"
      "v_cmp_gt_i32 vcc, 72, v7\n"
      "s_cbranch_vccnz .Lgm_Lw21\n"
      ".Lgm_Lw22:\n"
      "ds_read_b32 v7, v6 offset:32\n"
      "ds_read_b32 v28, v6 offset:40\n"
      "ds_read2_b64 v[32:35], v27 offset0:8 offset1:12\n"
      "s_waitcnt lgkmcnt(0)\n"
      "v_cmp_gt_i32 vcc, 1, v7\n"
      "s_cbranch_vccnz .Lgm_Lw22\n"
      "v_mfma_f32_16x16x32_bf16 v[48:51], v[32:35], v[80:83], 0\n"
      "v_mfma_f32_16x16x32_bf16 v[52:55], v[32:35], v[84:87], 0\n"
      "v_sub_f32 v29, 1.0, v28\n"
      "s_nop 6\n"
      "v_mov_b32_dpp v48, v52 quad_perm:[0,1,2,3] row_mask:0xa bank_mask:0xf\n"
      "v_mul_f32 v48, v29, v48\n"
      ".Lgm_Lw24:\n"
      "ds_read_b32 v7, v6 offset:0\n"
      "ds_read_b64_tr_b16 v[64:65], v15 offset:0\n"
      "ds_read_b64_tr_b16 v[66:67], v15 offset:4224\n"
      "ds_read_b64_tr_b16 v[68:69], v15 offset:32\n"
      "ds_read_b64_tr_b16 v[70:71], v15 offset:4256\n"
      "ds_read_b64_tr_b16 v[72:73], v15 offset:8448\n"
      "ds_read_b64_tr_b16 v[74:75], v15 offset:12672\n"
      "ds_read_b64_tr_b16 v[76:77], v15 offset:8480\n"
      "ds_read_b64_tr_b16 v[78:79], v15 offset:12704\n"
      "ds_read_b32 v30, v6 offset:36\n"
      "ds_read2_b64 v[32:35], v27 offset0:16 offset1:20\n"
      "ds_read2_b64 v[36:39], v27 offset0:24 offset1:28\n"
      "s_waitcnt lgkmcnt(0)\n"
      "v_cmp_gt_i32 vcc, 88, v7\n"
      "s_cbranch_vccnz .Lgm_Lw24\n"
      "v_cmp_ne_u32 vcc, 0, v30\n"
      "s_cbranch_vccnz .Lgm_Lw23\n"
      ".Lgm_Lw25:\n"
      "ds_read_b32 v7, v6 offset:36\n"
      "ds_read2_b64 v[32:35], v27 offset0:16 offset1:20\n"
      "ds_read2_b64 v[36:39], v27 offset0:24 offset1:28\n"
      "s_waitcnt lgkmcnt(0)\n"
      "v_cmp_gt_i32 vcc, 1, v7\n"
      "s_cbranch_vccnz .Lgm_Lw25\n"
      ".Lgm_Lw23:\n"
      "v_mfma_f32_16x16x32_bf16 v[52:55], v[32:35], v[64:67], 0\n"
      "v_mfma_f32_16x16x32_bf16 v[56:59], v[32:35], v[68:71], 0\n"
      "v_mfma_f32_16x16x32_bf16 v[52:55], v[36:39], v[72:75], v[52:55]\n"
      "v_mfma_f32_16x16x32_bf16 v[56:59], v[36:39], v[76:79], v[56:59]\n"
      "s_nop 7\n"
      "v_mov_b32_dpp v52, v56 quad_perm:[0,1,2,3] row_mask:0xa bank_mask:0xf\n"
      "v_fmac_f32 v48, v28, v52\n"
      "v_bfi_b32 v48, s7, v9, v48\n"
      "buffer_store_dword v48, v25, s[32:35], s49 offen sc1\n"
      "s_cmp_lt_u32 s5, 24\n"
      "s_cbranch_scc1 .Lgm_LAEND\n"
      "s_cmp_ge_u32 s6, 1\n"
      "s_cbranch_scc1 .Lgm_LAEND\n"
      "s_sleep 8\n"
      "buffer_load_dword v64, v16, s[32:35], s16 offen offset:0 sc1\n"
      "buffer_load_dword v65, v16, s[32:35], s16 offen offset:1024 sc1\n"
      "buffer_load_dword v66, v16, s[32:35], s16 offen offset:2048 sc1\n"
      "buffer_load_dword v67, v16, s[32:35], s16 offen offset:3072 sc1\n"
      "buffer_load_dword v68, v16, s[32:35], s17 offen offset:0 sc1\n"
      "buffer_load_dword v69, v16, s[32:35], s17 offen offset:1024 sc1\n"
      "buffer_load_dword v70, v16, s[32:35], s17 offen offset:2048 sc1\n"
      "buffer_load_dword v71, v16, s[32:35], s17 offen offset:3072 sc1\n"
      "s_sleep 2\n"
      "s_mov_b32 s39, 2097152\n"
      ".Lgm_Lp26:\n"
      "s_sub_u32 s39, s39, 1\n"
      "s_cbranch_scc1 .Lgm_Ld27\n"
      "buffer_load_dword v72, v16, s[32:35], s16 offen offset:0 sc1\n"
      "buffer_load_dword v73, v16, s[32:35], s16 offen offset:1024 sc1\n"
      "buffer_load_dword v74, v16, s[32:35], s16 offen offset:2048 sc1\n"
      "buffer_load_dword v75, v16, s[32:35], s16 offen offset:3072 sc1\n"
      "buffer_load_dword v76, v16, s[32:35], s17 offen offset:0 sc1\n"
      "buffer_load_dword v77, v16, s[32:35], s17 offen offset:1024 sc1\n"
      "buffer_load_dword v78, v16, s[32:35], s17 offen offset:2048 sc1\n"
      "buffer_load_dword v79, v16, s[32:35], s17 offen offset:3072 sc1\n"
      "s_waitcnt vmcnt(8)\n"
      "v_xor_b32 v64, s37, v64\n"
      "v_xor_b32 v65, s37, v65\n"
      "v_xor_b32 v66, s37, v66\n"
      "v_xor_b32 v67, s37, v67\n"
      "v_xor_b32 v68, s37, v68\n"
      "v_xor_b32 v69, s37, v69\n"
      "v_xor_b32 v70, s37, v70\n"
      "v_xor_b32 v71, s37, v71\n"
      "v_or3_b32 v7, v64, v65, v66\n"
      "v_or3_b32 v7, v7, v67, v68\n"
      "v_or3_b32 v7, v7, v69, v70\n"
      "v_or_b32 v7, v7, v71\n"
      "v_and_b32 v7, 0xff, v7\n"
      "v_cmp_ne_u32 vcc, 0, v7\n"
      "v_pk_add_f32 v[28:29], v[64:65], v[66:67]\n"
      "v_pk_add_f32 v[32:33], v[68:69], v[70:71]\n"
      "s_cbranch_vccz .Lgm_Ld27\n"
      "buffer_load_dword v64, v16, s[32:35], s16 offen offset:0 sc1\n"
      "buffer_load_dword v65, v16, s[32:35], s16 offen offset:1024 sc1\n"
      "buffer_load_dword v66, v16, s[32:35], s16 offen offset:2048 sc1\n"
      "buffer_load_dword v67, v16, s[32:35], s16 offen offset:3072 sc1\n"
      "buffer_load_dword v68, v16, s[32:35], s17 offen offset:0 sc1\n"
      "buffer_load_dword v69, v16, s[32:35], s17 offen offset:1024 sc1\n"
      "buffer_load_dword v70, v16, s[32:35], s17 offen offset:2048 sc1\n"
      "buffer_load_dword v71, v16, s[32:35], s17 offen offset:3072 sc1\n"
      "s_waitcnt vmcnt(8)\n"
      "v_xor_b32 v72, s37, v72\n"
      "v_xor_b32 v73, s37, v73\n"
      "v_xor_b32 v74, s37, v74\n"
      "v_xor_b32 v75, s37, v75\n"
      "v_xor_b32 v76, s37, v76\n"
      "v_xor_b32 v77, s37, v77\n"
      "v_xor_b32 v78, s37, v78\n"
      "v_xor_b32 v79, s37, v79\n"
      "v_or3_b32 v7, v72, v73, v74\n"
      "v_or3_b32 v7, v7, v75, v76\n"
      "v_or3_b32 v7, v7, v77, v78\n"
      "v_or_b32 v7, v7, v79\n"
      "v_and_b32 v7, 0xff, v7\n"
      "v_cmp_ne_u32 vcc, 0, v7\n"
      "v_pk_add_f32 v[28:29], v[72:73], v[74:75]\n"
      "v_pk_add_f32 v[32:33], v[76:77], v[78:79]\n"
      "s_cbranch_vccnz .Lgm_Lp26\n"
      ".Lgm_Ld27:\n"
      "v_pk_add_f32 v[28:29], v[28:29], v[32:33]\n"
      "v_add_f32 v28, v28, v29\n"
      "v_mov_b32 v29, v28\n"
      "s_nop 1\n"
      "v_permlane32_swap_b32 v28, v29\n"
      "v_add_f32 v28, v28, v29\n"
      "v_mov_b32 v29, v28\n"
      "s_nop 1\n"
      "v_permlane16_swap_b32 v28, v29\n"
      "v_add_f32 v28, v28, v29\n"
      "s_nop 1\n"
      "v_add_f32_dpp v28, v28, v28 row_ror:8 row_mask:0xf bank_mask:0xf bound_ctrl:1\n"
      "s_mov_b64 exec, 0xff\n"
      "v_cvt_pk_bf16_f32 v28, v28, v28\n"
      "buffer_store_short v28, v17, s[24:27], s18 offen\n"
      ".Lgm_LAEND:\n"
      "s_endpgm\n"
      ".p2align 12\n"
      ".Lgm_LTG:\n"
      "s_branch .Lgm_LA\n"
      ".Lgm_LG:\n"
      "s_barrier\n"
      "s_waitcnt lgkmcnt(0)\n"
      "s_add_u32 s36, s36, 2\n"
      "v_mov_b32 v9, s36\n"
      "ds_write_b32 v6, v9 offset:44\n"
      "ds_write_b32 v6, v8 offset:12\n"
      "v_and_b32 v11, 31, v0\n"
      "v_lshlrev_b32 v12, 2, v11\n"
      "v_lshlrev_b32 v11, 7, v11\n"
      "v_lshrrev_b32 v7, 5, v1\n"
      "v_lshl_add_u32 v11, v7, 2, v11\n"
      "v_lshlrev_b32 v13, 10, v3\n"
      "v_lshl_add_u32 v13, v2, 3, v13\n"
      "v_lshlrev_b32 v16, 2, v2\n"
      "v_and_b32 v7, 1, v0\n"
      "v_lshlrev_b32 v15, 6, v7\n"
      "v_lshl_add_u32 v15, v3, 4, v15\n"
      "v_and_b32 v7, 1, v0\n"
      "v_cmp_eq_u32 vcc, 1, v7\n"
      "v_mov_b32 v17, 0x0c0c0100\n"
      "v_mov_b32 v18, 0x01000c0c\n"
      "v_mov_b32 v19, 0x0c0c0c0c\n"
      "v_cndmask_b32 v17, v17, v18, vcc\n"
      "v_mov_b32 v24, 0x0c0c0302\n"
      "v_mov_b32 v18, 0x03020c0c\n"
      "v_cndmask_b32 v18, v24, v18, vcc\n"
      "v_cmp_lt_u32 vcc, 1, v5\n"
      "s_nop 1\n"
      "v_cndmask_b32 v20, v17, v19, vcc\n"
      "v_cndmask_b32 v21, v19, v17, vcc\n"
      "v_cndmask_b32 v22, v18, v19, vcc\n"
      "v_cndmask_b32 v23, v19, v18, vcc\n"
      "s_lshr_b32 s28, s41, 1\n"
      "s_addk_i32 s28, 104\n"
      "s_lshl_b32 s28, s28, 13\n"
      "s_and_b32 s7, s41, 1\n"
      "s_lshl_b32 s7, s7, 12\n"
      "s_add_u32 s28, s28, s7\n"
      "s_add_u32 s29, s43, 104\n"
      "s_lshl_b32 s29, s29, 13\n"
      "s_lshl_b32 s7, s42, 12\n"
      "s_add_u32 s29, s29, s7\n"
      "s_lshl_b32 s7, s5, 7\n"
      "s_add_u32 s29, s29, s7\n"
      "s_lshl_b32 s6, s4, 16\n"
      "s_add_u32 s6, s6, s7\n"
      "s_lshl_b32 s7, s42, 12\n"
      "s_add_u32 s6, s6, s7\n"
      "s_add_u32 s6, s6, 212992\n"
      "s_add_u32 s24, s32, s6\n"
      "s_addc_u32 s25, s33, 0\n"
      "s_and_b32 s7, s5, 3\n"
      "s_lshl_b32 s7, s7, 12\n"
      "s_bfe_u32 s10, s5, 0x20001\n"
      "s_lshl2_add_u32 s10, s41, s10\n"
      "s_addk_i32 s10, 13\n"
      "s_lshl_b32 s10, s10, 14\n"
      "s_add_u32 s10, s10, s7\n"
      "s_getreg_b32 s26, hwreg(HW_REG_XCC_ID, 2, 1)\n"
      "v_lshlrev_b32 v27, 14, v3\n"
      "v_bfe_u32 v9, v0, 3, 2\n"
      "v_lshl_or_b32 v27, v9, 12, v27\n"
      "v_and_b32 v9, 7, v0\n"
      "v_lshl_or_b32 v27, v9, 4, v27\n"
      "s_lshl_b32 s31, s4, 16\n"
      "s_lshl_b32 s7, s5, 7\n"
      "s_add_u32 s31, s31, s7\n"
      "s_add_u32 s31, s31, 212992\n"
      "s_add_u32 s44, s31, 8192\n"
      "v_mov_b32 v36, -1\n"
      "v_mov_b32 v37, -1\n"
      "v_mov_b32 v38, -1\n"
      "v_mov_b32 v39, -1\n"
      "s_and_b32 s20, s5, 1\n"
      "s_mul_i32 s21, s20, 24576\n"
      "s_xor_b32 s22, s21, 24576\n"
      "s_mov_b32 s6, 0\n"
      ".Lgm_Lt28:\n"
      "ds_read_b32 v7, v6 offset:0\n"
      "s_memrealtime s[86:87]\n"
      "s_waitcnt lgkmcnt(0)\n"
      "s_sub_u32 s39, s86, s84\n"
      "s_cmp_ge_i32 s39, 65\n"
      "s_cbranch_scc1 .Lgm_Lu29\n"
      "v_cmp_gt_i32 vcc, 88, v7\n"
      "s_cbranch_vccnz .Lgm_Lt28\n"
      ".Lgm_Lu29:\n"
      "v_and_b32 v9, 7, v0\n"
      "v_lshlrev_b32 v10, 8, v9\n"
      "buffer_load_dword v28, v10, s[32:35], 0 offen offset:1024 sc1\n"
      "s_sub_u32 s7, s86, 20\n"
      "v_mov_b32 v29, s7\n"
      "s_xor_b32 s30, s4, 1\n"
      "buffer_store_dwordx4 v[36:39], v27, s[32:35], s31 offen sc1\n"
      "s_waitcnt vmcnt(0)\n"
      "v_sub_u32 v30, s86, v28\n"
      "v_add_u32 v30, 100, v30\n"
      "v_cmp_gt_u32 vcc, 500, v30\n"
      "s_nop 1\n"
      "v_cndmask_b32 v28, v29, v28, vcc\n"
      "v_subrev_u32 v28, s84, v28\n"
      "s_nop 0\n"
      "v_readlane_b32 s7, v28, s30\n"
      "v_max_i32_dpp v29, v28, v28 quad_perm:[1,0,3,2] row_mask:0xf bank_mask:0xf bound_ctrl:1\n"
      "v_add_u32_dpp v28, v28, v28 quad_perm:[1,0,3,2] row_mask:0xf bank_mask:0xf bound_ctrl:1\n"
      "s_max_i32 s7, s7, 0\n"
      "v_max_i32_dpp v29, v29, v29 quad_perm:[2,3,0,1] row_mask:0xf bank_mask:0xf bound_ctrl:1\n"
      "v_add_u32_dpp v28, v28, v28 quad_perm:[2,3,0,1] row_mask:0xf bank_mask:0xf bound_ctrl:1\n"
      "v_mov_b32 v31, s7\n"
      "v_max_i32_dpp v29, v29, v29 row_ror:4 row_mask:0xf bank_mask:0xf bound_ctrl:1\n"
      "v_add_u32_dpp v28, v28, v28 row_ror:4 row_mask:0xf bank_mask:0xf bound_ctrl:1\n"
      "v_mul_i32_i24 v30, 32, v29\n"
      "v_mad_i32_i24 v30, v28, 4, v30\n"
      "v_ashrrev_i32 v30, 6, v30\n"
      "ds_write_b64 v6, v[30:31] offset:280\n"
      "v_mul_i32_i24 v29, 64, v29\n"
      "v_mad_i32_i24 v29, v28, 0, v29\n"
      "v_ashrrev_i32 v29, 6, v29\n"
      "ds_write_b32 v6, v29 offset:292\n"
      "s_waitcnt vmcnt(0)\n"
      "ds_write_b32 v6, v8 offset:288\n"
      "s_waitcnt lgkmcnt(0)\n"
      "s_load_dword s16, s[24:25], s21\n"
      "s_load_dword s17, s[24:25], s22\n"
      "s_add_u32 s24, s24, 0x4000\n"
      "s_addc_u32 s25, s25, 0\n"
      "s_waitcnt lgkmcnt(1)\n"
      "s_nop 7\n"
      "s_getreg_b32 s7, hwreg(HW_REG_IB_STS, 8, 1)\n"
      "s_xor_b32 s7, s7, 1\n"
      "s_add_u32 s6, s6, s7\n"
      "s_waitcnt lgkmcnt(0)\n"
      "s_cmp_gt_u32 s6, 0\n"
      "s_cselect_b32 s6, 1, 0\n"
      "s_xor_b32 s6, s6, s20\n"
      "s_xor_b32 s27, s6, s26\n"
      "s_andn2_b32 s7, s36, 1\n"
      "s_or_b32 s6, s7, s27\n"
      "v_mov_b32 v9, s6\n"
      "s_mov_b32 exec_hi, 0\n"
      "buffer_store_dword v9, v12, s[32:35], s29 offen sc1\n"
      "s_mov_b64 exec, -1\n"
      ".Lgm_Lt30:\n"
      "ds_read_b32 v7, v6 offset:0\n"
      "ds_read_b32 v61, v6 offset:292\n"
      "s_memrealtime s[86:87]\n"
      "s_waitcnt lgkmcnt(0)\n"
      "s_sub_u32 s39, s86, s84\n"
      "v_readfirstlane_b32 s87, v61\n"
      "s_sub_u32 s39, s39, s87\n"
      "s_cmp_ge_i32 s39, 322\n"
      "s_cbranch_scc1 .Lgm_Lu31\n"
      "v_cmp_gt_i32 vcc, 64, v7\n"
      "s_cbranch_vccnz .Lgm_Lt30\n"
      ".Lgm_Lu31:\n"
      "s_mov_b32 s39, 2097152\n"
      "s_branch .Lgm_Lc34\n"
      ".Lgm_Lp32:\n"
      "s_sub_u32 s39, s39, 1\n"
      "s_cbranch_scc1 .Lgm_Ld33\n"
      "s_sleep 4\n"
      ".Lgm_Lc34:\n"
      "buffer_load_dword v28, v11, s[32:35], s28 offen sc1\n"
      "s_waitcnt vmcnt(0)\n"
      "v_xor_b32 v28, s7, v28\n"
      "v_cmp_lt_u32 vcc, 1, v28\n"
      "s_cbranch_vccnz .Lgm_Lp32\n"
      ".Lgm_Ld33:\n"
      "v_cmp_eq_u32 vcc, 1, v28\n"
      "s_bcnt1_i32_b64 s30, vcc\n"
      "s_cmp_gt_u32 s30, 32\n"
      "s_cselect_b32 s38, 1, 0\n"
      "s_xor_b32 s38, s38, s26\n"
      "s_lshl_b32 s38, s38, 13\n"
      "s_or_b32 s6, s38, 1\n"
      "v_mov_b32 v9, s6\n"
      "ds_write_b32 v6, v9 offset:256\n"
      ".Lgm_Lw35:\n"
      "ds_read_b32 v7, v6 offset:8\n"
      "s_waitcnt lgkmcnt(0)\n"
      "v_cmp_gt_i32 vcc, 1, v7\n"
      "s_cbranch_vccnz .Lgm_Lw35\n"
      ".Lgm_Lt36:\n"
      "ds_read_b32 v7, v6 offset:0\n"
      "ds_read_b32 v61, v6 offset:292\n"
      "s_memrealtime s[86:87]\n"
      "s_waitcnt lgkmcnt(0)\n"
      "s_sub_u32 s39, s86, s84\n"
      "v_readfirstlane_b32 s87, v61\n"
      "s_sub_u32 s39, s39, s87\n"
      "s_cmp_ge_i32 s39, 436\n"
      "s_cbranch_scc1 .Lgm_Lu37\n"
      "v_cmp_gt_i32 vcc, 88, v7\n"
      "s_cbranch_vccnz .Lgm_Lt36\n"
      ".Lgm_Lu37:\n"
      "buffer_load_dwordx2 v[32:33], v13, s[32:35], s10 offen offset:0 sc1\n"
      "buffer_load_dwordx2 v[34:35], v13, s[32:35], s10 offen offset:128 sc1\n"
      "buffer_load_dwordx2 v[36:37], v13, s[32:35], s10 offen offset:256 sc1\n"
      "buffer_load_dwordx2 v[38:39], v13, s[32:35], s10 offen offset:384 sc1\n"
      "buffer_load_dwordx2 v[40:41], v13, s[32:35], s10 offen offset:512 sc1\n"
      "buffer_load_dwordx2 v[42:43], v13, s[32:35], s10 offen offset:640 sc1\n"
      "buffer_load_dwordx2 v[44:45], v13, s[32:35], s10 offen offset:768 sc1\n"
      "buffer_load_dwordx2 v[46:47], v13, s[32:35], s10 offen offset:896 sc1\n"
      ".Lgm_Lw38:\n"
      "ds_read_b32 v7, v6 offset:260\n"
      "ds_read_b128 v[28:31], v15 offset:1792\n"
      "s_waitcnt lgkmcnt(0)\n"
      "v_cmp_gt_i32 vcc, 1, v7\n"
      "s_cbranch_vccnz .Lgm_Lw38\n"
      "v_perm_b32 v48, v6, v28, v20\n"
      "v_perm_b32 v49, v6, v28, v21\n"
      "v_perm_b32 v50, v6, v28, v22\n"
      "v_perm_b32 v51, v6, v28, v23\n"
      "v_perm_b32 v52, v6, v29, v20\n"
      "v_perm_b32 v53, v6, v29, v21\n"
      "v_perm_b32 v54, v6, v29, v22\n"
      "v_perm_b32 v55, v6, v29, v23\n"
      "v_perm_b32 v56, v6, v30, v20\n"
      "v_perm_b32 v57, v6, v30, v21\n"
      "v_perm_b32 v58, v6, v30, v22\n"
      "v_perm_b32 v59, v6, v30, v23\n"
      "v_perm_b32 v60, v6, v31, v20\n"
      "v_perm_b32 v61, v6, v31, v21\n"
      "v_perm_b32 v62, v6, v31, v22\n"
      "v_perm_b32 v63, v6, v31, v23\n"
      "buffer_load_dwordx2 v[64:65], v13, s[32:35], s10 offen offset:0 sc1\n"
      "buffer_load_dwordx2 v[66:67], v13, s[32:35], s10 offen offset:128 sc1\n"
      "buffer_load_dwordx2 v[68:69], v13, s[32:35], s10 offen offset:256 sc1\n"
      "buffer_load_dwordx2 v[70:71], v13, s[32:35], s10 offen offset:384 sc1\n"
      "buffer_load_dwordx2 v[72:73], v13, s[32:35], s10 offen offset:512 sc1\n"
      "buffer_load_dwordx2 v[74:75], v13, s[32:35], s10 offen offset:640 sc1\n"
      "buffer_load_dwordx2 v[76:77], v13, s[32:35], s10 offen offset:768 sc1\n"
      "buffer_load_dwordx2 v[78:79], v13, s[32:35], s10 offen offset:896 sc1\n"
      "s_mov_b32 s39, 2097152\n"
      "s_branch .Lgm_Lc41\n"
      ".Lgm_Lp39:\n"
      "s_sub_u32 s39, s39, 1\n"
      "s_cbranch_scc1 .Lgm_Ld40\n"
      "buffer_load_dwordx2 v[32:33], v13, s[32:35], s10 offen offset:0 sc1\n"
      "buffer_load_dwordx2 v[34:35], v13, s[32:35], s10 offen offset:128 sc1\n"
      "buffer_load_dwordx2 v[36:37], v13, s[32:35], s10 offen offset:256 sc1\n"
      "buffer_load_dwordx2 v[38:39], v13, s[32:35], s10 offen offset:384 sc1\n"
      "buffer_load_dwordx2 v[40:41], v13, s[32:35], s10 offen offset:512 sc1\n"
      "buffer_load_dwordx2 v[42:43], v13, s[32:35], s10 offen offset:640 sc1\n"
      "buffer_load_dwordx2 v[44:45], v13, s[32:35], s10 offen offset:768 sc1\n"
      "buffer_load_dwordx2 v[46:47], v13, s[32:35], s10 offen offset:896 sc1\n"
      "s_waitcnt vmcnt(8)\n"
      "ds_read_b32 v26, v6 offset:268\n"
      "ds_read_b64 v[24:25], v6 offset:272\n"
      "v_mfma_f32_16x16x32_bf16 v[28:31], v[48:51], v[64:67], 0\n"
      "v_max3_u32 v7, v64, v65, v66\n"
      "v_max3_u32 v7, v7, v67, v68\n"
      "v_mfma_f32_16x16x32_bf16 v[28:31], v[52:55], v[68:71], v[28:31]\n"
      "v_max3_u32 v7, v7, v69, v70\n"
      "v_max3_u32 v7, v7, v71, v72\n"
      "v_mfma_f32_16x16x32_bf16 v[28:31], v[56:59], v[72:75], v[28:31]\n"
      "v_max3_u32 v7, v7, v73, v74\n"
      "v_max3_u32 v7, v7, v75, v76\n"
      "v_mfma_f32_16x16x32_bf16 v[28:31], v[60:63], v[76:79], v[28:31]\n"
      "v_max3_u32 v7, v7, v77, v78\n"
      "v_max_u32 v7, v7, v79\n"
      "s_waitcnt lgkmcnt(0)\n"
      "v_add_u32 v26, -1, v26\n"
      "v_or_b32 v7, v7, v26\n"
      "v_cmp_eq_u32 vcc, -1, v7\n"
      "s_cbranch_vccz .Lgm_Ld40\n"
      "buffer_load_dwordx2 v[64:65], v13, s[32:35], s10 offen offset:0 sc1\n"
      "buffer_load_dwordx2 v[66:67], v13, s[32:35], s10 offen offset:128 sc1\n"
      "buffer_load_dwordx2 v[68:69], v13, s[32:35], s10 offen offset:256 sc1\n"
      "buffer_load_dwordx2 v[70:71], v13, s[32:35], s10 offen offset:384 sc1\n"
      "buffer_load_dwordx2 v[72:73], v13, s[32:35], s10 offen offset:512 sc1\n"
      "buffer_load_dwordx2 v[74:75], v13, s[32:35], s10 offen offset:640 sc1\n"
      "buffer_load_dwordx2 v[76:77], v13, s[32:35], s10 offen offset:768 sc1\n"
      "buffer_load_dwordx2 v[78:79], v13, s[32:35], s10 offen offset:896 sc1\n"
      ".Lgm_Lc41:\n"
      "s_waitcnt vmcnt(8)\n"
      "ds_read_b32 v26, v6 offset:268\n"
      "ds_read_b64 v[24:25], v6 offset:272\n"
      "v_mfma_f32_16x16x32_bf16 v[28:31], v[48:51], v[32:35], 0\n"
      "v_max3_u32 v7, v32, v33, v34\n"
      "v_max3_u32 v7, v7, v35, v36\n"
      "v_mfma_f32_16x16x32_bf16 v[28:31], v[52:55], v[36:39], v[28:31]\n"
      "v_max3_u32 v7, v7, v37, v38\n"
      "v_max3_u32 v7, v7, v39, v40\n"
      "v_mfma_f32_16x16x32_bf16 v[28:31], v[56:59], v[40:43], v[28:31]\n"
      "v_max3_u32 v7, v7, v41, v42\n"
      "v_max3_u32 v7, v7, v43, v44\n"
      "v_mfma_f32_16x16x32_bf16 v[28:31], v[60:63], v[44:47], v[28:31]\n"
      "v_max3_u32 v7, v7, v45, v46\n"
      "v_max_u32 v7, v7, v47\n"
      "s_waitcnt lgkmcnt(0)\n"
      "v_add_u32 v26, -1, v26\n"
      "v_or_b32 v7, v7, v26\n"
      "v_cmp_eq_u32 vcc, -1, v7\n"
      "s_cbranch_vccnz .Lgm_Lp39\n"
      ".Lgm_Ld40:\n"
      "s_mov_b64 exec, 0xffff\n"
      "v_pk_mul_f32 v[28:29], v[28:29], v[24:25]\n"
      "v_pk_mul_f32 v[30:31], v[30:31], v[24:25]\n"
      "v_cvt_pk_bf16_f32 v28, v28, v30\n"
      "v_cvt_pk_bf16_f32 v29, v29, v31\n"
      "ds_write2_b32 v16, v28, v29 offset0:32 offset1:48\n"
      "ds_write_b32 v6, v8 offset:36\n"
      "s_endpgm\n"
      ".p2align 6\n"
      ".Lgm_LTF:\n"
      "s_branch .Lgm_LA\n"
      ".Lgm_LF:\n"
      "s_load_dwordx2 s[16:17], s[0:1], 48\n"
      "s_load_dwordx2 s[20:21], s[0:1], 56\n"
      "s_load_dwordx2 s[24:25], s[0:1], 24\n"
      "v_lshrrev_b32 v9, 3, v1\n"
      "v_and_b32 v7, 7, v0\n"
      "v_bitop3_b32 v7, v9, v7, 6 bitop3:0x6c\n"
      "v_mul_u32_u24 v9, 3584, v9\n"
      "v_lshl_add_u32 v64, v7, 4, v9\n"
      "v_add_u32 v65, 27616, v64\n"
      "v_add_u32 v66, 55232, v64\n"
      "v_add_u32 v67, 82848, v64\n"
      "s_cmp_lt_u32 s5, 28\n"
      "s_cselect_b32 s26, 0x1c0000, 0\n"
      "s_mov_b32 s27, s35\n"
      "s_mul_i32 s28, s4, 229376\n"
      "s_lshl_b32 s7, s5, 7\n"
      "s_add_u32 s28, s28, s7\n"
      "s_add_u32 s29, s28, 114688\n"
      "s_mov_b64 s[18:19], s[14:15]\n"
      "s_movk_i32 s22, 3584\n"
      "s_mov_b32 s23, s35\n"
      "v_lshlrev_b32 v24, 4, v1\n"
      "v_and_b32 v25, 7, v0\n"
      "v_lshl_add_u32 v25, v25, 2, v3\n"
      "v_lshlrev_b32 v25, 1, v25\n"
      "s_lshl1_add_u32 s46, s5, s42\n"
      "s_lshl_b32 s6, s46, 6\n"
      "s_movk_i32 m0, 25088\n"
      "s_waitcnt lgkmcnt(0)\n"
      "buffer_load_dwordx4 v64, s[24:27], s28 offen nt lds\n"
      "buffer_load_dwordx4 v65, s[24:27], s28 offen offset:1056 nt lds\n"
      "buffer_load_dwordx4 v66, s[24:27], s28 offen offset:2112 nt lds\n"
      "buffer_load_dwordx4 v67, s[24:27], s28 offen offset:3168 nt lds\n"
      "s_addk_i32 m0, 4224\n"
      "s_nop 0\n"
      "buffer_load_dwordx4 v64, s[24:27], s29 offen nt lds\n"
      "buffer_load_dwordx4 v65, s[24:27], s29 offen offset:1056 nt lds\n"
      "buffer_load_dwordx4 v66, s[24:27], s29 offen offset:2112 nt lds\n"
      "buffer_load_dwordx4 v67, s[24:27], s29 offen offset:3168 nt lds\n"
      "s_barrier\n"
      "s_mov_b32 s30, s6\n"
      "v_lshl_add_u32 v9, v3, 2, v4\n"
      "v_and_b32 v14, 7, v9\n"
      "v_lshrrev_b32 v15, 3, v9\n"
      "v_mul_u32_u24 v15, 1056, v15\n"
      "v_lshl_add_u32 v15, v14, 7, v15\n"
      "v_lshl_add_u32 v15, v5, 3, v15\n"
      "v_add_u32 v15, 25088, v15\n"
      "v_and_b32 v14, 6, v9\n"
      "v_xor_b32 v9, 0, v14\n"
      "v_lshl_add_u32 v10, v9, 4, v15\n"
      "v_xor_b32 v9, 2, v14\n"
      "v_lshl_add_u32 v11, v9, 4, v15\n"
      "v_xor_b32 v9, 4, v14\n"
      "v_lshl_add_u32 v12, v9, 4, v15\n"
      "v_xor_b32 v9, 6, v14\n"
      "v_lshl_add_u32 v13, v9, 4, v15\n"
      "s_lshl_b32 s6, s4, 7\n"
      "s_addk_i32 s6, 3072\n"
      "v_lshl_add_u32 v15, v3, 3, s6\n"
      ".Lgm_Lw42:\n"
      "ds_read_b32 v7, v6 offset:12\n"
      "ds_read_b32 v9, v6 offset:44\n"
      "s_waitcnt lgkmcnt(0)\n"
      "v_cmp_gt_i32 vcc, 1, v7\n"
      "s_cbranch_vccnz .Lgm_Lw42\n"
      "v_readfirstlane_b32 s36, v9\n"
      "s_and_b32 s37, s36, 0xff\n"
      "s_load_dword s48, s[12:13], 0\n"
      "s_waitcnt lgkmcnt(0)\n"
      "s_movk_i32 m0, 3072\n"
      "s_nop 0\n"
      "buffer_load_dwordx4 v24, s[12:15], 0 offen lds\n"
      "s_load_dword s48, s[16:17], 0\n"
      "s_load_dword s49, s[20:21], 0\n"
      "s_cmp_lt_u32 s5, 28\n"
      "s_cbranch_scc0 .Lgm_LFGATE\n"
      "s_lshl_b32 s6, s5, 4\n"
      "s_add_u32 s6, s6, s4\n"
      "s_addk_i32 s6, 5760\n"
      "s_lshl_b32 s6, s6, 7\n"
      "s_andn2_b32 s47, s6, 0x380\n"
      "s_lshl_b32 s7, s42, 10\n"
      "s_add_u32 s47, s47, s7\n"
      "v_lshrrev_b32 v9, 5, v1\n"
      "v_mul_u32_u24 v14, 1024, v9\n"
      "v_and_b32 v9, 31, v0\n"
      "v_lshl_add_u32 v14, v9, 2, v14\n"
      "s_mov_b32 s44, s6\n"
      "s_movk_i32 s7, 0xff\n"
      "v_mov_b32 v9, s37\n"
      "s_waitcnt vmcnt(0)\n"
      "ds_read2_b64 v[16:19], v15 offset0:0 offset1:4\n"
      "ds_read_b64_tr_b16 v[32:33], v10 offset:0\n"
      "ds_read_b64_tr_b16 v[34:35], v10 offset:2112\n"
      "ds_read_b64_tr_b16 v[36:37], v11 offset:0\n"
      "ds_read_b64_tr_b16 v[38:39], v11 offset:2112\n"
      "ds_read_b64_tr_b16 v[40:41], v12 offset:0\n"
      "ds_read_b64_tr_b16 v[42:43], v12 offset:2112\n"
      "ds_read_b64_tr_b16 v[44:45], v13 offset:0\n"
      "ds_read_b64_tr_b16 v[46:47], v13 offset:2112\n"
      "s_waitcnt lgkmcnt(6)\n"
      "v_mfma_f32_16x16x32_bf16 v[48:51], v[16:19], v[32:35], 0\n"
      "s_waitcnt lgkmcnt(4)\n"
      "v_mfma_f32_16x16x32_bf16 v[52:55], v[16:19], v[36:39], 0\n"
      "s_waitcnt lgkmcnt(2)\n"
      "v_mfma_f32_16x16x32_bf16 v[56:59], v[16:19], v[40:43], 0\n"
      "s_waitcnt lgkmcnt(0)\n"
      "v_mfma_f32_16x16x32_bf16 v[60:63], v[16:19], v[44:47], 0\n"
      "s_waitcnt vmcnt(0)\n"
      "ds_read2_b64 v[20:23], v15 offset0:8 offset1:12\n"
      "ds_read_b64_tr_b16 v[32:33], v10 offset:4224\n"
      "ds_read_b64_tr_b16 v[34:35], v10 offset:6336\n"
      "ds_read_b64_tr_b16 v[36:37], v11 offset:4224\n"
      "ds_read_b64_tr_b16 v[38:39], v11 offset:6336\n"
      "ds_read_b64_tr_b16 v[40:41], v12 offset:4224\n"
      "ds_read_b64_tr_b16 v[42:43], v12 offset:6336\n"
      "ds_read_b64_tr_b16 v[44:45], v13 offset:4224\n"
      "ds_read_b64_tr_b16 v[46:47], v13 offset:6336\n"
      "s_waitcnt lgkmcnt(6)\n"
      "v_mfma_f32_16x16x32_bf16 v[48:51], v[20:23], v[32:35], v[48:51]\n"
      "s_waitcnt lgkmcnt(4)\n"
      "v_mfma_f32_16x16x32_bf16 v[52:55], v[20:23], v[36:39], v[52:55]\n"
      "s_waitcnt lgkmcnt(2)\n"
      "v_mfma_f32_16x16x32_bf16 v[56:59], v[20:23], v[40:43], v[56:59]\n"
      "s_waitcnt lgkmcnt(0)\n"
      "v_mfma_f32_16x16x32_bf16 v[60:63], v[20:23], v[44:47], v[60:63]\n"
      "s_nop 3\n"
      "v_mov_b32_dpp v48, v52 quad_perm:[0,1,2,3] row_mask:0x2 bank_mask:0xf\n"
      "s_nop 1\n"
      "v_mov_b32_dpp v48, v56 quad_perm:[0,1,2,3] row_mask:0x4 bank_mask:0xf\n"
      "s_nop 1\n"
      "v_mov_b32_dpp v48, v60 quad_perm:[0,1,2,3] row_mask:0x8 bank_mask:0xf\n"
      "v_bfi_b32 v48, s7, v9, v48\n"
      "buffer_store_dword v48, v14, s[32:35], s44 offen sc1\n"
      ".Lgm_LFGATE:\n"
      "s_waitcnt lgkmcnt(0)\n"
      "buffer_load_dwordx4 v[28:31], v24, s[16:19], 0 offen\n"
      "buffer_load_ushort v26, v25, s[20:23], s30 offen\n"
      "v_mov_b32 v27, 0\n"
      "s_waitcnt vmcnt(0)\n"
      "ds_read_b128 v[32:35], v24 offset:3072\n"
      "s_waitcnt lgkmcnt(0)\n"
      "v_dot2c_f32_bf16 v27, v32, v28\n"
      "v_dot2c_f32_bf16 v27, v33, v29\n"
      "v_dot2c_f32_bf16 v27, v34, v30\n"
      "v_dot2c_f32_bf16 v27, v35, v31\n"
      "s_nop 2\n"
      "v_add_f32_dpp v27, v27, v27 quad_perm:[1,0,3,2] row_mask:0xf bank_mask:0xf bound_ctrl:1\n"
      "s_nop 1\n"
      "v_add_f32_dpp v27, v27, v27 quad_perm:[2,3,0,1] row_mask:0xf bank_mask:0xf bound_ctrl:1\n"
      "s_nop 1\n"
      "v_add_f32_dpp v27, v27, v27 row_ror:4 row_mask:0xf bank_mask:0xf bound_ctrl:1\n"
      "s_nop 1\n"
      "v_add_f32_dpp v27, v27, v27 row_ror:8 row_mask:0xf bank_mask:0xf bound_ctrl:1\n"
      "v_mov_b32 v28, v27\n"
      "s_nop 1\n"
      "v_permlane16_swap_b32 v27, v28\n"
      "v_add_f32 v27, v27, v28\n"
      "v_mov_b32 v28, v27\n"
      "s_nop 1\n"
      "v_permlane32_swap_b32 v27, v28\n"
      "v_add_f32 v27, v27, v28\n"
      "v_mul_f32 v27, 0xbfb8aa3b, v27\n"
      "v_exp_f32 v27, v27\n"
      "s_nop 0\n"
      "v_add_f32 v27, 1.0, v27\n"
      "v_rcp_f32 v27, v27\n"
      "s_nop 0\n"
      "ds_write_b32 v6, v27 offset:40\n"
      "s_cmp_lt_u32 s5, 28\n"
      "v_mov_b32 v27, 0\n"
      "s_cbranch_scc0 .Lgm_LFDONE\n"
      ".Lgm_Lt43:\n"
      "ds_read_b32 v7, v6 offset:0\n"
      "ds_read_b32 v61, v6 offset:292\n"
      "s_memrealtime s[86:87]\n"
      "s_waitcnt lgkmcnt(0)\n"
      "s_sub_u32 s39, s86, s84\n"
      "v_readfirstlane_b32 s87, v61\n"
      "s_sub_u32 s39, s39, s87\n"
      "s_cmp_ge_i32 s39, 300\n"
      "s_cbranch_scc1 .Lgm_Lu44\n"
      "v_cmp_gt_i32 vcc, 64, v7\n"
      "s_cbranch_vccnz .Lgm_Lt43\n"
      ".Lgm_Lu44:\n"
      "s_mov_b32 s39, 2097152\n"
      "s_branch .Lgm_Lc47\n"
      ".Lgm_Lp45:\n"
      "s_sub_u32 s39, s39, 1\n"
      "s_cbranch_scc1 .Lgm_Ld46\n"
      "s_sleep 4\n"
      ".Lgm_Lc47:\n"
      "buffer_load_dwordx4 v[28:31], v24, s[32:35], s47 offen sc1\n"
      "s_waitcnt vmcnt(0)\n"
      "v_xor_b32 v28, s37, v28\n"
      "v_xor_b32 v29, s37, v29\n"
      "v_xor_b32 v30, s37, v30\n"
      "v_xor_b32 v31, s37, v31\n"
      "v_or3_b32 v7, v28, v29, v30\n"
      "v_or_b32 v7, v7, v31\n"
      "v_and_b32 v7, 0xff, v7\n"
      "v_cmp_ne_u32 vcc, 0, v7\n"
      "s_cbranch_vccnz .Lgm_Lp45\n"
      ".Lgm_Ld46:\n"
      "v_permlane32_swap_b32 v28, v30\n"
      "v_permlane32_swap_b32 v29, v31\n"
      "v_add_f32 v28, v28, v30\n"
      "v_add_f32 v29, v29, v31\n"
      "v_lshlrev_b32 v26, 16, v26\n"
      "s_nop 0\n"
      "v_permlane16_swap_b32 v28, v29\n"
      "v_add_f32 v28, v28, v29\n"
      "s_nop 1\n"
      "v_add_f32_dpp v28, v28, v28 row_ror:8 row_mask:0xf bank_mask:0xf bound_ctrl:1\n"
      "v_add_f32 v28, v28, v26\n"
      "s_mov_b32 s6, 0xbfb8aa3b\n"
      "v_mul_f32_e64 v29, |v28|, s6\n"
      "v_exp_f32 v29, v29\n"
      "v_cmp_lt_f32 vcc, 0, v28\n"
      "v_mul_f32 v30, v29, v29\n"
      "s_nop 0\n"
      "v_cndmask_b32_e64 v31, v30, 1.0, vcc\n"
      "v_cndmask_b32_e64 v30, 1.0, v30, vcc\n"
      "v_fma_f32 v31, 2.0, v29, v31\n"
      "v_fma_f32 v30, 2.0, v30, v31\n"
      "v_rcp_f32 v30, v30\n"
      "v_mul_f32 v28, v28, v31\n"
      "v_mul_f32 v28, v28, v30\n"
      "v_cvt_pk_bf16_f32 v27, v28, v28\n"
      ".Lgm_LFDONE:\n"
      "ds_write_b16 v25, v27 offset:64\n"
      "ds_write_b32 v6, v8 offset:32\n"
      "s_cmpk_eq_u32 s2, 255\n"
      "s_cbranch_scc0 .Lgm_LFEND\n"
      "v_lshlrev_b32 v10, 7, v1\n"
      "s_andn2_b32 s7, s36, 1\n"
      "s_mov_b32 s44, 851968\n"
      "s_mov_b32 s45, 860160\n"
      "s_mov_b32 s46, 868352\n"
      "s_mov_b32 s47, 876544\n"
      ".Lgm_Lt48:\n"
      "ds_read_b32 v7, v6 offset:0\n"
      "ds_read_b32 v61, v6 offset:292\n"
      "s_memrealtime s[86:87]\n"
      "s_waitcnt lgkmcnt(0)\n"
      "s_sub_u32 s39, s86, s84\n"
      "v_readfirstlane_b32 s87, v61\n"
      "s_sub_u32 s39, s39, s87\n"
      "s_cmp_ge_i32 s39, 322\n"
      "s_cbranch_scc1 .Lgm_Lu49\n"
      "v_cmp_gt_i32 vcc, 64, v7\n"
      "s_cbranch_vccnz .Lgm_Lt48\n"
      ".Lgm_Lu49:\n"
      "s_mov_b32 s39, 2097152\n"
      "s_branch .Lgm_Lc52\n"
      ".Lgm_Lp50:\n"
      "s_sub_u32 s39, s39, 1\n"
      "s_cbranch_scc1 .Lgm_Ld51\n"
      "s_sleep 4\n"
      ".Lgm_Lc52:\n"
      "buffer_load_dwordx2 v[28:29], v10, s[32:35], s44 offen sc1\n"
      "buffer_load_dwordx2 v[30:31], v10, s[32:35], s45 offen sc1\n"
      "buffer_load_dwordx2 v[32:33], v10, s[32:35], s46 offen sc1\n"
      "buffer_load_dwordx2 v[34:35], v10, s[32:35], s47 offen sc1\n"
      "s_waitcnt vmcnt(0)\n"
      "v_xor_b32 v28, s7, v28\n"
      "v_xor_b32 v29, s7, v29\n"
      "v_xor_b32 v30, s7, v30\n"
      "v_xor_b32 v31, s7, v31\n"
      "v_xor_b32 v32, s7, v32\n"
      "v_xor_b32 v33, s7, v33\n"
      "v_xor_b32 v34, s7, v34\n"
      "v_xor_b32 v35, s7, v35\n"
      "v_or3_b32 v7, v28, v29, v30\n"
      "v_or3_b32 v7, v7, v31, v32\n"
      "v_or3_b32 v7, v7, v33, v34\n"
      "v_or_b32 v7, v7, v35\n"
      "v_cmp_lt_u32 vcc, 1, v7\n"
      "s_cbranch_vccnz .Lgm_Lp50\n"
      ".Lgm_Ld51:\n"
      "s_mov_b64 exec, 1\n"
      "v_mov_b32 v28, s36\n"
      "buffer_store_dword v28, v6, s[32:35], 0 offen sc1\n"
      ".Lgm_LFEND:\n"
      "s_endpgm\n"
      :
      : "{s[0:1]}"(__builtin_amdgcn_kernarg_segment_ptr()), "{s2}"(__builtin_amdgcn_workgroup_id_x()), "{v0}"(__builtin_amdgcn_workitem_id_x()),
        "i"((__attribute__((address_space(3))) char*)smem)
      : "v1", "v2", "v3", "v4", "v5", "v6", "v7", "v8", "v9", "v10", "v11", "v12", "v13", "v14", "v15", "v16", "v17", "v18", "v19", "v20", "v21", "v22", "v23", "v24", "v25", "v26", "v27", "v28", "v29", "v30", "v31", "v32", "v33", "v34", "v35", "v36", "v37", "v38", "v39", "v40", "v41", "v42", "v43", "v44", "v45", "v46", "v47", "v48", "v49", "v50", "v51", "v52", "v53", "v54", "v55", "v56", "v57", "v58", "v59", "v60", "v61", "v62", "v63", "v64", "v65", "v66", "v67", "v68", "v69", "v70", "v71", "v72", "v73", "v74", "v75", "v76", "v77", "v78", "v79", "v80", "v81", "v82", "v83", "v84", "v85", "v86", "v87", "v88", "v89", "v90", "v91", "v92", "v93", "v94", "v95", "v96", "v97", "v98", "v99", "s3", "s4", "s5", "s6", "s7", "s8", "s9", "s10", "s11", "s12", "s13", "s14", "s15", "s16", "s17", "s18", "s19", "s20", "s21", "s22", "s23", "s24", "s25", "s26", "s27", "s28", "s29", "s30", "s31", "s33", "s34", "s35", "s36", "s37", "s38", "s39", "s40", "s41", "s42", "s43", "s44", "s45", "s46", "s47", "s48", "s49", "s50", "s51", "s52", "s53", "s54", "s55", "s56", "s57", "s58", "s59", "s60", "s61", "s62", "s63", "s64", "s65", "s66", "s67", "s68", "s69", "s70", "s71", "s72", "s73", "s74", "s75", "s76", "s77", "s78", "s79", "s80", "s81", "s82", "s83", "s84", "s85", "s86", "s87", "s88", "s89", "s90", "s91", "s92", "s93", "s94", "s95", "vcc", "scc", "memory");
#endif
}

extern "C" void kernel_launch(void* const* d_in, const int* in_sizes, int n_in, void* d_out, int out_size, void* d_ws, size_t ws_size, hipStream_t stream) {
  gated_mix_fused<<<256, 512, 0, stream>>>(d_in[3], d_in[0], d_ws, d_in[6], d_in[1], d_in[2], d_in[9], d_in[7], d_in[4], d_in[8], d_out);
}
```
